```python
import torch

def _softmax(x, axis=-1):
    return torch.softmax(x, dim=axis)

def _gelu(x):
    return torch.nn.functional.gelu(x, approximate='tanh')

def _silu(x):
    return torch.nn.functional.silu(x)

def _sigmoid(x):
    return torch.sigmoid(x)

def _layernorm(x, axis=-1, eps=1e-05):
    m = x.mean(dim=axis, keepdim=True)
    v = x.var(dim=axis, keepdim=True, unbiased=False)
    return (x - m) / torch.sqrt(v + eps)

def _scatter_add(tgt, idx, src, axis=0):
    return tgt.clone().index_add_(axis, idx, src)

def _segment_sum(src, seg, n):
    out = torch.zeros((n,) + src.shape[1:], dtype=src.dtype)
    return out.index_add_(0, seg, src)

def _pad(x, pw):
    flat = tuple((v for pair in reversed(pw) for v in pair))
    return torch.nn.functional.pad(x, flat)
kernel_name = 'adaLN_cond__linear_kernel_hedgehog__glu_gated_bilinear__prompt'

def setup_inputs():

    _w = lambda a: (lambda t: t.to(torch.bfloat16) if t.is_floating_point() else t)(torch.as_tensor(a))

    def _A_setup():
        B, S, D, H = (1, 2048, 448, 16)
        Dh = D // H
        return {'x': _w((lambda *s: torch.randn(*s, dtype=torch.bfloat16) * 0.05)(B, S, D)), 'wqkv': _w((lambda *s: torch.randn(*s, dtype=torch.bfloat16) * 0.4)(D, 3 * D)), 'w_phi': _w((lambda *s: torch.randn(*s, dtype=torch.bfloat16) * 0.4)(H, Dh, Dh) * Dh ** (-0.5)), 'wo': _w((lambda *s: torch.randn(*s, dtype=torch.bfloat16) * 0.05)(D, D))}
    _A_in = _A_setup()

    def _B_setup():
        B, S, D, F = (1, 2048, 448, 1792)
        return {'x': _w((lambda *s: torch.randn(*s, dtype=torch.bfloat16) * 0.05)(B, S, D)), 'w_gate': _w((lambda *s: torch.randn(*s, dtype=torch.bfloat16) * 1.6)(D, F) * D ** (-0.5)), 'w_up': _w((lambda *s: torch.randn(*s, dtype=torch.bfloat16) * 1.6)(D, F) * D ** (-0.5)), 'w_down': _w((lambda *s: torch.randn(*s, dtype=torch.bfloat16) * 1.6)(F, D) * F ** (-0.5))}
    _B_in = _B_setup()
    return {'A_x': _A_in['x'], 'A_wqkv': _A_in['wqkv'], 'A_w_phi': _A_in['w_phi'], 'A_wo': _A_in['wo'], 'B_x': _B_in['x'], 'B_w_gate': _B_in['w_gate'], 'B_w_up': _B_in['w_up'], 'B_w_down': _B_in['w_down'], 'c': _w((lambda *s: torch.randn(*s, dtype=torch.bfloat16) * 0.1)(1, 448) * 8.0), 'w_mod': _w((lambda *s: torch.randn(*s, dtype=torch.bfloat16) * 0.1)(448, 6 * 448) * 448 ** (-0.5) * 8.0)}

def reference(A_x, A_wqkv, A_w_phi, A_wo, B_x, B_w_gate, B_w_up, B_w_down, c, w_mod):

    def _A_ref(x, wqkv, w_phi, wo):
        B, S, D = x.shape
        H = 16
        Dh = D // H
        qkv = (lambda x, *d: x.permute(*d))((x @ wqkv).reshape(B, S, 3, H, Dh), 2, 0, 3, 1, 4)
        q, k, v = (qkv[0], qkv[1], qkv[2])
        pq = torch.einsum('bhsd,hdf->bhsf', q, w_phi)
        pk = torch.einsum('bhsd,hdf->bhsf', k, w_phi)
        phi_q = torch.concatenate([_softmax(pq, axis=-1), _softmax(-pq, axis=-1)], axis=-1)
        phi_k = torch.concatenate([_softmax(pk, axis=-1), _softmax(-pk, axis=-1)], axis=-1)
        kv = torch.einsum('bhsf,bhsd->bhfd', phi_k, v)
        z = torch.einsum('bhsf->bhf', phi_k)
        num = torch.einsum('bhqf,bhfd->bhqd', phi_q, kv)
        den = torch.einsum('bhqf,bhf->bhq', phi_q, z)[..., None] + 1e-06
        o = num / den
        return (lambda x, *d: x.permute(*d))(o, 0, 2, 1, 3).reshape(B, S, D) @ wo

    def _B_ref(x, w_gate, w_up, w_down):
        g = torch.einsum('bsd,df->bsf', x, w_gate)
        u = torch.einsum('bsd,df->bsf', x, w_up)
        h = g * u
        return torch.einsum('bsf,fd->bsd', h, w_down)
    x = A_x
    mod = (_silu(c) @ w_mod).reshape(c.shape[0], 6, 1, -1)
    sa, ca, ga, sb, cb, gb = (mod[:, 0].clone(), mod[:, 1].clone(), mod[:, 2].clone(), mod[:, 3].clone(), mod[:, 4].clone(), mod[:, 5].clone())
    h = _layernorm(x, axis=-1) * (1 + ca) + sa
    x = x + ga * _A_ref(h, A_wqkv, A_w_phi, A_wo)
    h = _layernorm(x, axis=-1) * (1 + cb) + sb
    x = x + gb * _B_ref(h, B_w_gate, B_w_up, B_w_down)
    return x
```

```cpp
#include <hip/hip_runtime.h>
#include <hip/hip_bf16.h>
typedef unsigned short u16;
__device__ __forceinline__ float bf2f(u16 v){ return __uint_as_float(((unsigned)v)<<16); }
__device__ __forceinline__ u16 f2bf(float f){ unsigned u=__float_as_uint(f); u += 0x7fffu + ((u>>16)&1u); return (u16)(u>>16); }
constexpr int S=2048, D=448, H=16, DH=28, F=1792;

__global__ void k_mod(const u16* c, const u16* wmod, float* mod){
  int n = blockIdx.x*blockDim.x+threadIdx.x; if(n>=6*D) return;
  float acc=0.f;
  for(int k=0;k<D;k++){ float x=bf2f(c[k]); float s=x/(1.f+__expf(-x)); acc += s*bf2f(wmod[k*6*D+n]); }
  mod[n]=acc;
}
template<bool XF32>
__global__ void k_ln(const void* xv, const float* shift, const float* scale, float* h){
  int t=blockIdx.x; __shared__ float red[2];
  __shared__ float xs[D];
  for(int k=threadIdx.x;k<D;k+=blockDim.x) xs[k]= XF32? ((const float*)xv)[t*D+k] : bf2f(((const u16*)xv)[t*D+k]);
  __syncthreads();
  if(threadIdx.x==0){ float m=0; for(int k=0;k<D;k++) m+=xs[k]; m/=D; float v=0; for(int k=0;k<D;k++){float d=xs[k]-m; v+=d*d;} v/=D; red[0]=m; red[1]=rsqrtf(v+1e-5f);}
  __syncthreads();
  for(int k=threadIdx.x;k<D;k+=blockDim.x) h[t*D+k]=(xs[k]-red[0])*red[1]*(1.f+scale[k])+shift[k];
}
__global__ void k_gemm(const float* A, const u16* B, float* C, int M,int N,int K){
  __shared__ float As[16][17], Bs[16][17];
  int tx=threadIdx.x&15, ty=threadIdx.x>>4;
  int row=blockIdx.y*16+ty, col=blockIdx.x*16+tx; float acc=0;
  for(int k0=0;k0<K;k0+=16){
    As[ty][tx]=A[row*K+k0+tx]; Bs[ty][tx]=bf2f(B[(k0+ty)*N+col]); __syncthreads();
    for(int k=0;k<16;k++) acc+=As[ty][k]*Bs[k][tx]; __syncthreads();
  }
  C[row*N+col]=acc;
}
__global__ void k_phi(const float* qkv, const u16* wphi, float* phi  ){
  int idx=blockIdx.x*blockDim.x+threadIdx.x; if(idx>=2*S*H) return;
  int h=idx%H, t=(idx/H)%S, w=idx/(H*S);
  const float* q=qkv+t*3*D+w*D+h*DH; float* out=phi+(size_t)idx*0; out=phi+(((size_t)w*S+t)*H+h)*56;
  float mx=-1e30f, mn=1e30f;
  for(int f=0;f<DH;f++){ float a=0; for(int d=0;d<DH;d++) a+=q[d]*bf2f(wphi[(h*DH+d)*DH+f]); out[f]=a; mx=fmaxf(mx,a); mn=fminf(mn,a);}
  float s1=0,s2=0;
  for(int f=0;f<DH;f++){ float a=out[f]; float e1=__expf(a-mx), e2=__expf(mn-a); out[f]=e1; out[28+f]=e2; s1+=e1; s2+=e2; }
  for(int f=0;f<DH;f++){ out[f]/=s1; out[28+f]/=s2; }
}
__global__ void k_kv(const float* qkv, const float* phi, float* kv  ){
  int h=blockIdx.x/56, f=blockIdx.x%56; int d=threadIdx.x; if(d>=29) return;
  const float* pk=phi+(size_t)S*H*56; float acc=0;
  for(int t=0;t<S;t++){ float p=pk[((size_t)t*H+h)*56+f]; float v= d<28? qkv[t*3*D+2*D+h*DH+d]:1.f; acc+=p*v; }
  kv[(h*56+f)*29+d]=acc;
}
__global__ void k_o(const float* phi, const float* kv, float* o){
  int idx=blockIdx.x*blockDim.x+threadIdx.x; if(idx>=S*D) return;
  int t=idx/D, c=idx%D, h=c/DH, d=c%DH; const float* pq=phi+((size_t)t*H+h)*56; float n=0,dn=0;
  for(int f=0;f<56;f++){ n+=pq[f]*kv[(h*56+f)*29+d]; dn+=pq[f]*kv[(h*56+f)*29+28]; }
  o[idx]=n/(dn+1e-6f);
}
__global__ void k_res(const u16* x, const float* a, const float* gate, float* x1){
  int idx=blockIdx.x*blockDim.x+threadIdx.x; if(idx>=S*D) return; x1[idx]=bf2f(x[idx])+gate[idx%D]*a[idx];
}
__global__ void k_mul(const float* g, const float* u, float* hh){ int idx=blockIdx.x*blockDim.x+threadIdx.x; if(idx<S*F) hh[idx]=g[idx]*u[idx]; }
__global__ void k_fin(const float* x1, const float* a, const float* gate, u16* out){
  int idx=blockIdx.x*blockDim.x+threadIdx.x; if(idx>=S*D) return; out[idx]=f2bf(x1[idx]+gate[idx%D]*a[idx]);
}
extern "C" void kernel_launch(void* const* d_in, const int* in_sizes, int n_in, void* d_out, int out_size, void* d_ws, size_t ws_size, hipStream_t stream){
  const u16* x=(const u16*)d_in[0]; const u16* wqkv=(const u16*)d_in[1]; const u16* wphi=(const u16*)d_in[2]; const u16* wo=(const u16*)d_in[3];
  const u16* wg=(const u16*)d_in[5]; const u16* wu=(const u16*)d_in[6]; const u16* wd=(const u16*)d_in[7]; const u16* c=(const u16*)d_in[8]; const u16* wmod=(const u16*)d_in[9];
  float* ws=(float*)d_ws; size_t off=0;
  float* mod=ws+off; off+=4096;
  float* h=ws+off; off+=S*D;
  float* qkv=ws+off; off+=S*3*D;
  float* phi=ws+off; off+=(size_t)2*S*H*56;
  float* kv=ws+off; off+=32768;
  float* o=ws+off; off+=S*D;
  float* a=ws+off; off+=S*D;
  float* x1=ws+off; off+=S*D;
  float* g=ws+off; off+=S*F;
  float* u=ws+off; off+=S*F;
  float* hh=ws+off; off+=S*F;
  k_mod<<<(6*D+63)/64,64,0,stream>>>(c,wmod,mod);
  k_ln<false><<<S,64,0,stream>>>(x,mod+0,mod+D,h);
  k_gemm<<<dim3(3*D/16,S/16),256,0,stream>>>(h,wqkv,qkv,S,3*D,D);
  k_phi<<<(2*S*H+63)/64,64,0,stream>>>(qkv,wphi,phi);
  k_kv<<<H*56,64,0,stream>>>(qkv,phi,kv);
  k_o<<<(S*D+255)/256,256,0,stream>>>(phi,kv,o);
  k_gemm<<<dim3(D/16,S/16),256,0,stream>>>(o,wo,a,S,D,D);
  k_res<<<(S*D+255)/256,256,0,stream>>>(x,a,mod+2*D,x1);
  k_ln<true><<<S,64,0,stream>>>(x1,mod+3*D,mod+4*D,h);
  k_gemm<<<dim3(F/16,S/16),256,0,stream>>>(h,wg,g,S,F,D);
  k_gemm<<<dim3(F/16,S/16),256,0,stream>>>(h,wu,u,S,F,D);
  k_mul<<<(S*F+255)/256,256,0,stream>>>(g,u,hh);
  k_gemm<<<dim3(D/16,S/16),256,0,stream>>>(hh,wd,a,S,D,F);
  k_fin<<<(S*D+255)/256,256,0,stream>>>(x1,a,mod+5*D,(u16*)d_out);
}
```

```cpp
#include <hip/hip_runtime.h>
#include <stdint.h>

typedef unsigned u32;
typedef unsigned short u16;
typedef unsigned long long u64;
typedef u32 u32x4 __attribute__((ext_vector_type(4), may_alias));
typedef u32 u32x2 __attribute__((ext_vector_type(2), may_alias));
typedef float f32x16 __attribute__((ext_vector_type(16)));
typedef float f32x4 __attribute__((ext_vector_type(4), may_alias));
typedef float f32x2 __attribute__((ext_vector_type(2), may_alias));
typedef __bf16 bf16x8 __attribute__((ext_vector_type(8)));
typedef __bf16 bf16x2 __attribute__((ext_vector_type(2)));
typedef short s16x4 __attribute__((ext_vector_type(4)));
#define DI __device__ __forceinline__
#define AS3 __attribute__((address_space(3)))
#define AS1 __attribute__((address_space(1)))
#define RLX_AGENT __ATOMIC_RELAXED, __HIP_MEMORY_SCOPE_AGENT

constexpr u32 WS_EPOCH = 0, WS_TMO = 256;
constexpr u32 WS_MODG = 4096;
constexpr u32 WS_KVF = 16384;
constexpr u32 WS_OF = 20480;
constexpr u32 WS_XF = 28672;
constexpr u32 WS_HF = 36864;
constexpr u32 WS_PF = 45056;
constexpr u32 WS_UF = 46080;
constexpr u32 WS_KVP = 65536;
constexpr u32 WS_OFR = WS_KVP + (1u << 20);
constexpr u32 WS_YF = WS_OFR + (2u << 20);
constexpr u32 WS_STAT = WS_YF + 64 * 28 * 1024;
constexpr u32 WS_HH = WS_STAT + 64 * 14 * 256;
constexpr u32 WS_WGU = WS_HH + 64 * 112 * 1024;
constexpr u32 WS_WD = WS_WGU + 3136 * 1024;
constexpr u32 WS_EXF = WS_WD + 1568 * 1024;
constexpr u32 WS_END = WS_EXF + 112 * 512;
constexpr u32 WS_DBG = 24u << 20;

constexpr u32 L_XR = 0, XRW = 28672;
constexpr u32 L_W1 = 114688;
constexpr u32 L_PHK = 114688, L_VIM = 131072, L_KVS = 139264;
constexpr u32 L_PHI = 151552;
constexpr u32 L_TS = 153600, L_TA = 155392;
constexpr u32 L_GA = 153600, L_S2 = 153856, L_GB = 154112, L_R2 = 154368;
constexpr u32 L_RED = 157184;
constexpr u32 L_WO = 57344;
constexpr u32 L_OT = 86016;
constexpr u32 L_P1T = 57344, L_P1R = 65536;
constexpr u32 SLOT = 28672;
constexpr u32 LDS_BYTES = 163840;

constexpr u32 SPIN_MAX = 100000;
constexpr u32 CODE_KB = 56;

DI f32x16 mfma(u32x4 a, u32x4 b, f32x16 c) {
  return __builtin_amdgcn_mfma_f32_32x32x16_bf16(__builtin_bit_cast(bf16x8, a), __builtin_bit_cast(bf16x8, b), c, 0, 0, 0);
}
DI float asf(u32 x) { return __builtin_bit_cast(float, x); }
DI u32 asu(float x) { return __builtin_bit_cast(u32, x); }
DI u32 cvtpk(float lo, float hi) { f32x2 v = {lo, hi}; return __builtin_bit_cast(u32, __builtin_convertvector(v, bf16x2)); }
DI float dot2(u32 a, u32 b, float c) { return __builtin_amdgcn_fdot2_f32_bf16(__builtin_bit_cast(bf16x2, a), __builtin_bit_cast(bf16x2, b), c, false); }
DI float blo(u32 d) { return asf(d << 16); }
DI float bhi(u32 d) { return asf(d & 0xffff0000u); }
DI u32x4 pack8(const f32x16& c, int o) { return u32x4{cvtpk(c[o], c[o + 1]), cvtpk(c[o + 2], c[o + 3]), cvtpk(c[o + 4], c[o + 5]), cvtpk(c[o + 6], c[o + 7])}; }
DI float swsum(float v) { auto r = __builtin_amdgcn_permlane32_swap(asu(v), asu(v), false, false); return asf(r[0]) + asf(r[1]); }
DI float swmax(float v) { auto r = __builtin_amdgcn_permlane32_swap(asu(v), asu(v), false, false); return fmaxf(asf(r[0]), asf(r[1])); }
DI float swmin(float v) { auto r = __builtin_amdgcn_permlane32_swap(asu(v), asu(v), false, false); return fminf(asf(r[0]), asf(r[1])); }
DI u32x4 mksrd(const void* p, u32 bytes) { u64 a = (u64)p; return u32x4{(u32)a, (u32)(a >> 32) & 0xffffu, bytes, 0x00020000u}; }
#define RSRC(p, bytes) __builtin_amdgcn_make_buffer_rsrc((void*)(p), 0, (bytes), 0x00020000)
#define WAITV(n) asm volatile("s_waitcnt vmcnt(" #n ")" ::: "memory")
#define BAR() asm volatile("s_waitcnt lgkmcnt(0)\n\ts_barrier" ::: "memory")
DI void glds(u32 m0, u32 voff, u32x4 srd, u32 soff) {
  asm volatile("s_mov_b32 m0, %0\n\ts_nop 0\n\tbuffer_load_dwordx4 %1, %2, %3 offen lds" ::"s"(m0), "v"(voff), "s"(srd), "s"(soff) : "memory");
}
DI void glds1(u32 m0, u32 voff, u32x4 srd, u32 soff) {
  asm volatile("s_mov_b32 m0, %0\n\ts_nop 0\n\tbuffer_load_dwordx4 %1, %2, %3 offen sc1 lds" ::"s"(m0), "v"(voff), "s"(srd), "s"(soff) : "memory");
}
#define G7_STEP(SC) "s_add_u32 m0, m0, %[dm]\n\ts_add_u32 %[so], %[so], %[dg]\n\tbuffer_load_dwordx4 %[v], %[srd], %[so] offen " SC "lds\n\t"
template <int SC1> DI void glds7(u32 m0, u32 voff, u32x4 srd, u32 soff, u32 dm, u32 dg) {
  if constexpr (SC1)
    asm volatile("s_mov_b32 m0, %[m]\n\ts_nop 0\n\tbuffer_load_dwordx4 %[v], %[srd], %[so] offen sc1 lds\n\t" G7_STEP("sc1 ") G7_STEP("sc1 ") G7_STEP("sc1 ") G7_STEP("sc1 ") G7_STEP("sc1 ") G7_STEP("sc1 ")
                 : [so] "+s"(soff) : [m] "s"(m0), [v] "v"(voff), [srd] "s"(srd), [dm] "s"(dm), [dg] "s"(dg) : "memory", "scc");
  else
    asm volatile("s_mov_b32 m0, %[m]\n\ts_nop 0\n\tbuffer_load_dwordx4 %[v], %[srd], %[so] offen lds\n\t" G7_STEP("") G7_STEP("") G7_STEP("") G7_STEP("") G7_STEP("") G7_STEP("")
                 : [so] "+s"(soff) : [m] "s"(m0), [v] "v"(voff), [srd] "s"(srd), [dm] "s"(dm), [dg] "s"(dg) : "memory", "scc");
}

#define STAMP(i)

DI void poll_flags(const char* ws, u32 off, bool act, u32 tag) {
  u32 spins = 0;
  for (;;) {
    u32 f = act ? __hip_atomic_load((AS1 u32*)(ws + off), RLX_AGENT) : tag;
    if (__all(f == tag)) break;
    if (++spins > SPIN_MAX) { __hip_atomic_store((AS1 u32*)(ws + WS_TMO), tag, RLX_AGENT); break; }
    __builtin_amdgcn_s_sleep(1);
  }
  asm volatile("" ::: "memory");
}
DI void set_flag(char* ws, u32 off, u32 tag) { __hip_atomic_store((AS1 u32*)(ws + off), tag, RLX_AGENT); }

DI void red16(float (&a)[16], float (&o)[4]) {
  float b[8], c[4];
#pragma unroll
  for (int i = 0; i < 8; i++) { auto r = __builtin_amdgcn_permlane32_swap(asu(a[i]), asu(a[i + 8]), false, false); b[i] = asf(r[0]) + asf(r[1]); }
#pragma unroll
  for (int i = 0; i < 4; i++) { auto r = __builtin_amdgcn_permlane16_swap(asu(b[i]), asu(b[i + 4]), false, false); c[i] = asf(r[0]) + asf(r[1]); }
#pragma unroll
  for (int i = 0; i < 4; i++) {
    float v = c[i];
    v += asf(__builtin_amdgcn_update_dpp(0, asu(v), 0x128, 0xf, 0xf, true));
    v += asf(__builtin_amdgcn_update_dpp(0, asu(v), 0x124, 0xf, 0xf, true));
    v += asf(__builtin_amdgcn_update_dpp(0, asu(v), 0x122, 0xf, 0xf, true));
    v += asf(__builtin_amdgcn_update_dpp(0, asu(v), 0x121, 0xf, 0xf, true));
    o[i] = v;
  }
}

DI void hedgehog(const f32x16& p, u32 hf, u32x4 (&fr)[4]) {
  constexpr float LG = 1.4426950408889634f;
  const float NINF = -__builtin_inff();
  float pp[16], pn[16];
#pragma unroll
  for (int r = 0; r < 16; r++) { pp[r] = p[r]; pn[r] = p[r]; }
#pragma unroll
  for (int r = 12; r < 16; r++) { pp[r] = hf ? NINF : p[r]; pn[r] = hf ? -NINF : p[r]; }
  float mx = pp[0], mn = pn[0];
#pragma unroll
  for (int r = 1; r < 16; r++) { mx = fmaxf(mx, pp[r]); mn = fminf(mn, pn[r]); }
  mx = swmax(mx); mn = swmin(mn);
  const float a = -mx * LG, b = mn * LG;
  float s0 = 0.f, s1 = 0.f;
#pragma unroll
  for (int r = 0; r < 16; r++) {
    pp[r] = __builtin_amdgcn_exp2f(__builtin_fmaf(pp[r], LG, a)); s0 += pp[r];
    pn[r] = __builtin_amdgcn_exp2f(__builtin_fmaf(pn[r], -LG, b)); s1 += pn[r];
  }
  s0 = __builtin_amdgcn_rcpf(swsum(s0)); s1 = __builtin_amdgcn_rcpf(swsum(s1));
#pragma unroll
  for (int k = 0; k < 2; k++) {
    fr[k] = u32x4{cvtpk(pp[8 * k] * s0, pp[8 * k + 1] * s0), cvtpk(pp[8 * k + 2] * s0, pp[8 * k + 3] * s0), cvtpk(pp[8 * k + 4] * s0, pp[8 * k + 5] * s0), cvtpk(pp[8 * k + 6] * s0, pp[8 * k + 7] * s0)};
    fr[2 + k] = u32x4{cvtpk(pn[8 * k] * s1, pn[8 * k + 1] * s1), cvtpk(pn[8 * k + 2] * s1, pn[8 * k + 3] * s1), cvtpk(pn[8 * k + 4] * s1, pn[8 * k + 5] * s1), cvtpk(pn[8 * k + 6] * s1, pn[8 * k + 7] * s1)};
  }
}

__global__ __launch_bounds__(256, 1) void fused_block(const u16* __restrict__ x, const u16* __restrict__ wqkv, const u16* __restrict__ wphi, const u16* __restrict__ wo,
                                                      const u16* __restrict__ wg, const u16* __restrict__ wu, const u16* __restrict__ wd, const u16* __restrict__ cvec,
                                                      const u16* __restrict__ wmod, u16* __restrict__ out, char* __restrict__ ws) {
  extern __shared__ __attribute__((aligned(16))) char lds_[];
  AS3 char* L = (AS3 char*)lds_;
  const u32 LB = (u32)(size_t)L;
#define LR128(off) (*(AS3 u32x4*)(L + (off)))
#define LR64(off) (*(AS3 u32x2*)(L + (off)))
#define LR32(off) (*(AS3 u32*)(L + (off)))
#define TRR(off) __builtin_bit_cast(u32x2, __builtin_amdgcn_ds_read_tr16_b64_v4i16((AS3 s16x4*)(L + (off))))
  const u32 tid = threadIdx.x, lane = tid & 63;
  const u32 w = __builtin_amdgcn_readfirstlane(tid >> 6);
  const u32 bid = blockIdx.x, mt = (bid & 7) * 2 + (bid >> 7), j = (bid >> 3) & 15;
  const u32 l31 = lane & 31, hf = lane >> 5;
  const u32 tq = (lane & 15) >> 2, tp_ = lane & 3, tblk = (lane >> 4) & 1;
  const u32x4 s_ws = mksrd(ws, WS_END);
  const auto r_ws = RSRC(ws, WS_END);
  STAMP(0);

  u32 vep, vc0, vc1;
  u32x4 wa0, wa1, wb0, wb1;
  {
    const u32x4 s_c = mksrd(cvec, 896), s_wm = mksrd(wmod, 448 * 5376);
    const u32 vo = tid * 5376 + bid * 32;
    asm volatile(
        "buffer_load_dwordx4 %3, %8, %11, 0 offen\n\t"
        "buffer_load_dwordx4 %4, %8, %11, 0 offen offset:16\n\t"
        "buffer_load_dwordx4 %5, %9, %11, 0 offen\n\t"
        "buffer_load_dwordx4 %6, %9, %11, 0 offen offset:16\n\t"
        "buffer_load_ushort %1, %7, %10, 0 offen\n\t"
        "buffer_load_ushort %2, %7, %10, 0 offen offset:512\n\t"
        "buffer_load_dword %0, off, %12, 0 sc1"
        : "=&v"(vep), "=&v"(vc0), "=&v"(vc1), "=&v"(wa0), "=&v"(wa1), "=&v"(wb0), "=&v"(wb1)
        : "v"(tid * 2), "v"(vo), "v"(vo + 256 * 5376), "s"(s_c), "s"(s_wm), "s"(s_ws)
        : "memory");
  }
  {
    const u64 pc = __builtin_amdgcn_s_getpc();
    const u32 piece = ((bid >> 3) * 4 + w) % CODE_KB;
    asm volatile("s_mov_b32 m0, %0\n\ts_nop 0\n\tglobal_load_lds_dwordx4 %1, off" ::"s"(LB + L_TS), "v"((pc & ~(u64)1023) + piece * 1024 + lane * 16) : "memory");
  }
  {
    const u32x4 s_x = mksrd(x, 2048 * 896);
    const u32 r = lane >> 3, ch = lane & 7;
    const u32 ve = r * 896 + ((ch ^ (r >> 1)) << 4), vo_ = ve ^ 64;
    const u32 so = (mt * 128 + w * 32) * 896, m = LB + L_XR + w * XRW;
    glds7<0>(m, ve, s_x, so, 1024, 128);
    glds7<0>(m + 7168, vo_, s_x, so + 7168, 1024, 128);
    glds7<0>(m + 14336, ve, s_x, so + 14336, 1024, 128);
    glds7<0>(m + 21504, vo_, s_x, so + 21504, 1024, 128);
  }
  const u32x4 s_w = mksrd(wqkv, 448 * 2688);
  u32 vw[3];
  const u32 winb = j * 56 - (j & 1) * 8;
#pragma unroll
  for (int m = 0; m < 3; m++) {
    const u32 ci = m * 64 + lane, row = (ci * 43691) >> 19, ch = ci - row * 12;
    vw[m] = row * 2688 + (ch >> 2) * 896 + (ch & 3) * 16;
  }
#pragma unroll
  for (int i = 0; i < 3; i++)
#pragma unroll
    for (int m = 0; m < 3; m++) glds(LB + L_W1 + (w * 3 + i) * 3072 + m * 1024, vw[m], s_w, (w * 3 + i) * 43008 + winb);
  if (w == 0) {
    const u32x4 s_ph = mksrd(wphi, 16 * 1568);
    glds(LB + L_PHI, j * 1568 + lane * 16, s_ph, 0);
    glds(LB + L_PHI + 1024, j * 1568 + lane * 16 + 1024, s_ph, 0);
  }

  asm volatile("s_waitcnt vmcnt(38)" : "+v"(vep), "+v"(vc0), "+v"(vc1), "+v"(wa0), "+v"(wa1), "+v"(wb0), "+v"(wb1)::"memory");
  const u32 tag = __builtin_amdgcn_readfirstlane(vep) * 5u + 0x3C6EF35Fu;
  STAMP(1);
  {
    const float c0 = blo(vc0), c1 = blo(vc1);
    const float si0 = c0 * __builtin_amdgcn_rcpf(1.f + __builtin_amdgcn_exp2f(-1.4426950408889634f * c0));
    const float si1 = c1 * __builtin_amdgcn_rcpf(1.f + __builtin_amdgcn_exp2f(-1.4426950408889634f * c1));
    const u32 sp = cvtpk(si0, si1);
    const u32 s0l = sp & 0xffffu, s0h = sp << 16, s1l = sp >> 16, s1h = sp & 0xffff0000u;
    float a[16], o[4];
#pragma unroll
    for (int i = 0; i < 4; i++) {
      a[2 * i] = dot2(wa0[i], s0l, dot2(wb0[i], s1l, 0.f));
      a[2 * i + 1] = dot2(wa0[i], s0h, dot2(wb0[i], s1h, 0.f));
      a[8 + 2 * i] = dot2(wa1[i], s0l, dot2(wb1[i], s1l, 0.f));
      a[8 + 2 * i + 1] = dot2(wa1[i], s0h, dot2(wb1[i], s1h, 0.f));
    }
    red16(a, o);
    const u32 p = lane >> 4;
    if ((lane & 15) == 0) *(AS3 f32x4*)(L + L_RED + w * 64 + ((p & 1) * 4 + (p >> 1) * 8) * 4) = f32x4{o[0], o[1], o[2], o[3]};
    BAR();
    if (tid < 8 && bid < 168) {
      f32x2 t = *(AS3 f32x2*)(L + L_RED + tid * 8);
#pragma unroll
      for (int i = 1; i < 4; i++) { f32x2 u = *(AS3 f32x2*)(L + L_RED + i * 64 + tid * 8); t += u; }
      const u64 g = ((u64)cvtpk(t[0], t[1]) << 32) | tag;
      __hip_atomic_store((AS1 u64*)(ws + WS_MODG + (bid * 8 + tid) * 8), g, RLX_AGENT);
    }
  }
  STAMP(2);

  WAITV(9);
  u32x4 xf[28];
  {
    const u32 sw = (l31 >> 1) & 7, b0 = L_XR + w * XRW + (l31 >> 3) * 7168 + (l31 & 7) * 128;
#pragma unroll
    for (int m = 0; m < 4; m++) {
      const u32 ad = b0 + (((2 * m + hf) ^ sw) << 4);
#pragma unroll
      for (int b = 0; b < 7; b++) xf[b * 4 + m] = LR128(ad + b * 1024);
    }
  }
  BAR();
  STAMP(3);
#pragma unroll
  for (int i = 0; i < 4; i++)
#pragma unroll
    for (int m = 0; m < 3; m++) glds(LB + L_XR + w * XRW + i * 3072 + m * 1024, vw[m], s_w, (12 + w * 4 + i) * 43008 + winb);
  const u32 njob = bid < 80 ? 2 : 1;
#pragma unroll 1
  for (u32 jb = 0; jb < njob; jb++) {
    const u32 id = bid + jb * 256, dst = LB + L_XR + jb * XRW + 12288;
    if (id < 224) {
      const u32 ks = id >> 3, fp = id & 7;
      const u32x4 s_g = mksrd(wg, 448 * 3584), s_u = mksrd(wu, 448 * 3584);
#pragma unroll
      for (int i = 0; i < 4; i++) {
        const u32 p = w + i * 4;
        const u32 pp = p >= 7 ? p - 7 : p, ci = pp * 64 + lane, row = (ci * 9363) >> 18, ch = ci - row * 28;
        const u32 vo = p < 14 ? (ks * 16 + row) * 3584 + fp * 448 + ch * 16 : 0x7fff0000u;
        if (p >= 7) glds(dst + p * 1024, vo, s_u, 0); else glds(dst + p * 1024, vo, s_g, 0);
      }
    } else {
      const u32x4 s_d = mksrd(wd, 1792 * 896);
#pragma unroll
      for (int i = 0; i < 4; i++) {
        const u32 p = w + i * 4;
        glds(dst + p * 1024, p < 14 ? lane * 16 + p * 1024 : 0x7fff0000u, s_d, (id - 224) * 14336);
      }
    }
  }

  float rs, nmr;
  {
    float su[2] = {0.f, 0.f}, sq[2] = {0.f, 0.f};
#pragma unroll
    for (int s = 0; s < 28; s++)
#pragma unroll
      for (int i = 0; i < 4; i++) { su[i & 1] = dot2(xf[s][i], 0x3F803F80u, su[i & 1]); sq[i & 1] = dot2(xf[s][i], xf[s][i], sq[i & 1]); }
    const float m = swsum(su[0] + su[1]) * (1.f / 448.f), e2 = swsum(sq[0] + sq[1]) * (1.f / 448.f);
    rs = __builtin_amdgcn_rsqf(fmaxf(e2 - m * m, 0.f) + 1e-5f);
    nmr = -m * rs;
  }
  u32x2 gsa, gca;
  const u32 gofs = WS_MODG + (tid < 224 ? tid : 0) * 8;
  asm volatile("buffer_load_dwordx2 %0, %2, %3, 0 offen sc1\n\tbuffer_load_dwordx2 %1, %2, %3, 0 offen offset:1792 sc1" : "=&v"(gsa), "=&v"(gca) : "v"(gofs), "s"(s_ws) : "memory");
#pragma unroll
  for (int s = 0; s < 28; s++)
#pragma unroll
    for (int i = 0; i < 4; i++) { const u32 d = xf[s][i]; xf[s][i] = cvtpk(__builtin_fmaf(blo(d), rs, nmr), __builtin_fmaf(bhi(d), rs, nmr)); }
  STAMP(4);
  asm volatile("s_waitcnt vmcnt(0)" : "+v"(gsa), "+v"(gca)::"memory");
  {
    u32 spins = 0;
    while (!__all(gsa[0] == tag && gca[0] == tag)) {
      if (++spins > SPIN_MAX) { set_flag(ws, WS_TMO, tag); break; }
      __builtin_amdgcn_s_sleep(1);
      const u64 a = __hip_atomic_load((AS1 u64*)(ws + gofs), RLX_AGENT), b = __hip_atomic_load((AS1 u64*)(ws + gofs + 1792), RLX_AGENT);
      gsa = u32x2{(u32)a, (u32)(a >> 32)}; gca = u32x2{(u32)b, (u32)(b >> 32)};
    }
    if (tid < 224) {
      const u32 sp = cvtpk(1.f + blo(gca[1]), 1.f + bhi(gca[1]));
      *(AS3 u32x2*)(L + L_TS + tid * 8) = u32x2{(sp & 0xffffu) * 0x10001u, (sp >> 16) * 0x10001u};
      *(AS3 u32x2*)(L + L_TA + tid * 8) = u32x2{gsa[1] << 16, gsa[1] & 0xffff0000u};
    }
  }
  BAR();
  STAMP(5);

  f32x16 aq = {}, ak = {}, av = {};
  {
    u32 mk[2][4];
    {
      const u32 jj = l31 & 7, on = ((l31 >> 3) & 1) == hf ? ((jj & 1) ? 0xffff0000u : 0xffffu) : 0u;
#pragma unroll
      for (int c = 0; c < 2; c++)
#pragma unroll
        for (int i = 0; i < 4; i++) mk[c][i] = ((l31 >> 4) == (u32)c && (jj >> 1) == (u32)i) ? on : 0u;
    }
    const u32 wl = (4 * hf + tq) * 192 + tblk * 32 + tp_ * 8;
#pragma unroll
    for (int be = 0; be < 14; be++) {
      const u32 sv = LR32(L_TS + (be * 32 + l31) * 4);
      f32x16 hh;
#pragma unroll
      for (int g = 0; g < 4; g++) {
        const f32x4 t = *(AS3 f32x4*)(L + L_TA + (be * 32 + g * 8) * 4 + hf * 16);
        hh[4 * g] = t[0]; hh[4 * g + 1] = t[1]; hh[4 * g + 2] = t[2]; hh[4 * g + 3] = t[3];
      }
#pragma unroll
      for (int c = 0; c < 2; c++) hh = mfma(u32x4{sv & mk[c][0], sv & mk[c][1], sv & mk[c][2], sv & mk[c][3]}, xf[2 * be + c], hh);
#pragma unroll
      for (int c = 0; c < 2; c++) {
        const int s = 2 * be + c;
        const u32 wb = (s < 12 ? L_W1 + s * 3072 : L_XR + ((s - 12) >> 2) * XRW + ((s - 12) & 3) * 3072);
        const u32x4 hb = pack8(hh, 8 * c);
        u32x2 a0, a1;
        a0 = TRR(wb + wl); a1 = TRR(wb + wl + 8 * 192);
        aq = mfma(u32x4{a0[0], a0[1], a1[0], a1[1]}, hb, aq);
        a0 = TRR(wb + wl + 64); a1 = TRR(wb + wl + 8 * 192 + 64);
        ak = mfma(u32x4{a0[0], a0[1], a1[0], a1[1]}, hb, ak);
        a0 = TRR(wb + wl + 128); a1 = TRR(wb + wl + 8 * 192 + 128);
        av = mfma(u32x4{a0[0], a0[1], a1[0], a1[1]}, hb, av);
      }
    }
  }
  BAR();
  STAMP(6);
  const u32 odd = j & 1;
  const bool dwg = j < 14;
  const u32 tph = j >= 7 ? 1 : 0, ng = j - tph * 7;
  if (dwg && w >= 2) {
    const u32x4 s_wo = mksrd(wo, 448 * 896);
    const u32 r = lane >> 3, vo = r * 896 + (((lane & 7) ^ (((r >> 1) & 1) << 2)) << 4);
    const u32 p0 = (w - 2) * 28;
#pragma unroll
    for (int i = 0; i < 4; i++) glds7<0>(LB + L_WO + (p0 + i * 7) * 1024, vo, s_wo, (p0 + i * 7) * 7168 + ng * 128, 1024, 7168);
  }
  u32x4 aph[2];
  {
#pragma unroll
    for (int s = 0; s < 2; s++) {
      u32x2 t[2];
#pragma unroll
      for (int u = 0; u < 2; u++) {
        int d = 16 * s + 8 * u + 4 * (int)hf + (int)tq - 4 * (int)odd;
        d = d < 0 ? 0 : (d > 27 ? 27 : d);
        t[u] = TRR(L_PHI + d * 56 + tblk * 32 + tp_ * 8);
      }
      aph[s] = u32x4{t[0][0], t[0][1], t[1][0], t[1][1]};
    }
    if (odd) { if (!hf) { aph[0][0] = 0; aph[0][1] = 0; } }
    else { if (hf) { aph[1][2] = 0; aph[1][3] = 0; } }
  }
  {
    f32x16 pk = {};
    pk = mfma(aph[0], pack8(ak, 0), pk);
    pk = mfma(aph[1], pack8(ak, 8), pk);
    u32x4 fk[4];
    hedgehog(pk, hf, fk);
    const u32 t = w * 32 + l31;
    const u32 swp = (l31 & 7) | ((((l31 >> 3) ^ (l31 >> 1)) & 1) << 3);
    const u32 pa = (L_PHK + t * 128) | ((hf ^ swp) << 3);
#pragma unroll
    for (int s = 0; s < 4; s++) {
      const u32 u0 = (s >> 1) * 8 + (s & 1) * 4;
      *(AS3 u32x2*)(L + (pa ^ (u0 << 3))) = u32x2{fk[s][0], fk[s][1]};
      *(AS3 u32x2*)(L + (pa ^ ((u0 + 2) << 3))) = u32x2{fk[s][2], fk[s][3]};
    }
    u32x4 v0 = pack8(av, 0), v1 = pack8(av, 8);
    if (odd) { if (!hf) { v0[0] = 0x3F80u; v0[1] = 0; } }
    else { if (hf) { v1[2] = 0x3F80u; v1[3] = 0; } }
    const u32 va = (L_VIM + t * 64) | ((hf ^ ((l31 >> 1) & 7)) << 3);
    *(AS3 u32x2*)(L + va) = u32x2{v0[0], v0[1]};
    *(AS3 u32x2*)(L + (va ^ 16)) = u32x2{v0[2], v0[3]};
    *(AS3 u32x2*)(L + (va ^ 32)) = u32x2{v1[0], v1[1]};
    *(AS3 u32x2*)(L + (va ^ 48)) = u32x2{v1[2], v1[3]};
  }
  BAR();
  STAMP(7);
  if (w < 2) {
    f32x16 kv = {};
    const u32 swa = tq | ((hf ^ (tq >> 1)) & 1) << 3;
    const u32 ua = w * 8 + tblk * 4 + tp_;
    const u32 a0 = L_PHK + (8 * hf + tq) * 128 + ((ua ^ swa) << 3), a1 = L_PHK + (8 * hf + 4 + tq) * 128 + ((ua ^ swa ^ 4) << 3);
    const u32 swb = 4 * hf + (tq >> 1), ub = tblk * 4 + tp_;
    const u32 b0 = L_VIM + (8 * hf + tq) * 64 + ((ub ^ swb) << 3), b1 = L_VIM + (8 * hf + 4 + tq) * 64 + ((ub ^ swb ^ 2) << 3);
#pragma unroll
    for (int kk = 0; kk < 8; kk++) {
      const u32x2 x0 = TRR(a0 + kk * 2048), x1 = TRR(a1 + kk * 2048), y0 = TRR(b0 + kk * 1024), y1 = TRR(b1 + kk * 1024);
      kv = mfma(u32x4{x0[0], x0[1], x1[0], x1[1]}, u32x4{y0[0], y0[1], y1[0], y1[1]}, kv);
    }
    const u32 base = WS_KVP + ((j * 4 + w * 2) * 16 + mt) * 1024 + lane * 16;
    __builtin_amdgcn_raw_buffer_store_b128(pack8(kv, 0), r_ws, base, 0, 16);
    __builtin_amdgcn_raw_buffer_store_b128(pack8(kv, 8), r_ws, base + 16384, 0, 16);
    WAITV(0);
    if (lane == 0) set_flag(ws, WS_KVF + (j * 2 + w) * 128 + mt * 4, tag);
  }
  STAMP(8);
  u32x4 fq[4];
  {
    f32x16 pq = {};
    pq = mfma(aph[0], pack8(aq, 0), pq);
    pq = mfma(aph[1], pack8(aq, 8), pq);
    hedgehog(pq, hf, fq);
  }
#pragma unroll 1
  for (u32 jb = 0; jb < njob; jb++) {
    const u32 id = bid + jb * 256, src = L_XR + jb * XRW + 12288;
    const u32 rowl = 4 * hf + tq;
#pragma unroll
    for (int i = 0; i < 4; i++) {
      const u32 fr = w + i * 4;
      if (fr < 14) {
        u32 ad, st, go;
        if (id < 224) {
          const u32 fl = fr >= 7 ? 1 : 0, tl = fr - fl * 7;
          ad = src + tblk * 7168 + rowl * 448 + fl * 224 + tl * 32 + tp_ * 8; st = 8 * 448;
          go = WS_WGU + ((((id & 7) * 2 + fl) * 28 + (id >> 3)) * 7 + tl) * 1024;
        } else {
          ad = src + rowl * 896 + fr * 64 + tblk * 32 + tp_ * 8; st = 8 * 896;
          go = WS_WD + (fr * 112 + (id - 224)) * 1024;
        }
        const u32x2 t0 = TRR(ad), t1 = TRR(ad + st);
        __builtin_amdgcn_raw_buffer_store_b128(u32x4{t0[0], t0[1], t1[0], t1[1]}, r_ws, go + lane * 16, 0, 16);
      }
    }
  }
  WAITV(0);
  BAR();
  if (tid == 0) set_flag(ws, WS_PF + bid * 4, tag);
  STAMP(9);

  {
    poll_flags(ws, WS_KVF + (j * 2 + (w >> 1)) * 128 + (lane & 15) * 4, lane < 16, tag);
    STAMP(10);
    u32x4 pt[16];
#pragma unroll
    for (int i = 0; i < 16; i++) pt[i] = __builtin_amdgcn_raw_buffer_load_b128(r_ws, lane * 16, WS_KVP + ((j * 4 + w) * 16 + ((i + mt) & 15)) * 1024, 16);
    float sm[8] = {};
    u32 one_lo = 0x00003F80u, one_hi = 0x3F800000u;
    asm("" : "+s"(one_lo), "+s"(one_hi));
#pragma unroll
    for (int i = 0; i < 16; i++)
#pragma unroll
      for (int e = 0; e < 4; e++) { sm[2 * e] = dot2(pt[i][e], one_lo, sm[2 * e]); sm[2 * e + 1] = dot2(pt[i][e], one_hi, sm[2 * e + 1]); }
    *(AS3 u32x4*)(L + L_KVS + w * 1024 + lane * 16) = u32x4{cvtpk(sm[0], sm[1]), cvtpk(sm[2], sm[3]), cvtpk(sm[4], sm[5]), cvtpk(sm[6], sm[7])};
    BAR();
    f32x16 o = {};
#pragma unroll
    for (int s = 0; s < 4; s++) o = mfma(LR128(L_KVS + s * 1024 + lane * 16), fq[s], o);
    auto dd = __builtin_amdgcn_permlane32_swap(asu(odd ? o[0] : o[12]), asu(odd ? o[0] : o[12]), false, false);
    const float inv = __builtin_amdgcn_rcpf(asf(odd ? dd[0] : dd[1]) + 1e-6f);
#pragma unroll
    for (int r = 0; r < 16; r++) o[r] *= inv;
    u32x4 o0 = pack8(o, 0), o1 = pack8(o, 8);
    if (odd) { if (!hf) { o0[0] = 0; o0[1] = 0; } }
    else { if (hf) { o1[2] = 0; o1[3] = 0; } }
    const u32 base = WS_OFR + (((mt * 4 + w) * 16 + j) * 2) * 1024 + lane * 16;
    __builtin_amdgcn_raw_buffer_store_b128(o0, r_ws, base, 0, 16);
    __builtin_amdgcn_raw_buffer_store_b128(o1, r_ws, base + 1024, 0, 16);
    WAITV(0);
    if (lane == 0) set_flag(ws, WS_OF + (mt * 4 + w) * 128 + j * 4, tag);
  }
  STAMP(11);
  if (bid == 0 && tid == 0) {   }

  const u32 nh = w & 1, th = w >> 1;
  u32x4 x1p[2];
  x1p[0] = u32x4{0, 0, 0, 0}; x1p[1] = x1p[0];
  {
    const u32 which = tid >> 5, e = tid & 31;
    if (dwg && which < 3) {
      const u32 go = WS_MODG + ((which == 0 ? 448 : which == 1 ? 896 : 1120) + ng * 32 + e) * 8;
      u32 spins = 0; u64 a;
      for (;;) {
        a = __hip_atomic_load((AS1 u64*)(ws + go), RLX_AGENT);
        if ((u32)a == tag) break;
        if (++spins > SPIN_MAX) { set_flag(ws, WS_TMO, tag); break; }
        __builtin_amdgcn_s_sleep(1);
      }
      const u32 d = (u32)(a >> 32);
      float lo = blo(d), hi = bhi(d);
      if (which == 1) { const u32 sp = cvtpk(1.f + lo, 1.f + hi); lo = blo(sp); hi = bhi(sp); }
      *(AS3 f32x2*)(L + (which == 0 ? L_GA : which == 1 ? L_S2 : L_GB) + e * 8) = f32x2{lo, hi};
    }
  }
  if (dwg) {
    u32x4 wf[32];
    {
      const u32 wl = L_WO + (4 * hf + tq) * 128 + (((nh * 4 + tblk * 2 + (tp_ >> 1)) ^ ((tq >> 1) << 2)) << 4) + (tp_ & 1) * 8;
#pragma unroll
      for (int h = 0; h < 16; h++)
#pragma unroll
        for (int s = 0; s < 2; s++) {
          const int row = 28 * h - 4 * (h & 1) + 16 * s;
          const u32x2 t0 = TRR(wl + row * 128), t1 = TRR(wl + (row + 8) * 128);
          wf[h * 2 + s] = u32x4{t0[0], t0[1], t1[0], t1[1]};
        }
    }
    u32x2 xr[4];
    {
      const auto r_x = RSRC(x, 2048 * 896);
      const u32 xo = (mt * 128 + tph * 64 + th * 32 + l31) * 896 + (ng * 64 + nh * 32 + 4 * hf) * 2;
#pragma unroll
      for (int g = 0; g < 4; g++) xr[g] = __builtin_amdgcn_raw_buffer_load_b64(r_x, xo + g * 16, 0, 0);
    }
    BAR();
    STAMP(12);
    if (w == 3) poll_flags(ws, WS_PF + lane * 4, true, tag), poll_flags(ws, WS_PF + 256 + lane * 4, true, tag), poll_flags(ws, WS_PF + 512 + lane * 4, true, tag), poll_flags(ws, WS_PF + 768 + lane * 4, true, tag);
    BAR();
#pragma unroll
    for (int i = 0; i < 3; i++) glds7<1>(LB + i * SLOT + w * 7168, lane * 16, s_ws, WS_WGU + j * 196 * 1024 + i * SLOT + w * 7168, 1024, 1024);
    poll_flags(ws, WS_OF + (mt * 4 + tph * 2 + th) * 128 + (nh * 8 + (lane & 7)) * 4, lane < 8, tag);
    STAMP(13);
    {
      const u32 so = WS_OFR + ((mt * 4 + tph * 2 + th) * 32 + nh * 16) * 1024, m = LB + L_OT + (th * 32 + nh * 16) * 1024;
#pragma unroll
      for (int i = 0; i < 16; i++) { const u32 pc = (i + ng * 2) & 15; glds1(m + pc * 1024, lane * 16, s_ws, so + pc * 1024); }
    }
    WAITV(0);
    BAR();
    STAMP(14);
    f32x16 acc = {};
#pragma unroll
    for (int ks = 0; ks < 32; ks++) acc = mfma(wf[ks], LR128(L_OT + (th * 32 + ks) * 1024 + lane * 16), acc);
    u32x4 yp[2];
    float su = 0.f, sq = 0.f;
#pragma unroll
    for (int g = 0; g < 4; g++) {
      const f32x4 ga = *(AS3 f32x4*)(L + L_GA + (nh * 32 + g * 8 + 4 * hf) * 4), s2 = *(AS3 f32x4*)(L + L_S2 + (nh * 32 + g * 8 + 4 * hf) * 4);
      const u32 p0 = cvtpk(__builtin_fmaf(ga[0], acc[4 * g], blo(xr[g][0])), __builtin_fmaf(ga[1], acc[4 * g + 1], bhi(xr[g][0])));
      const u32 p1 = cvtpk(__builtin_fmaf(ga[2], acc[4 * g + 2], blo(xr[g][1])), __builtin_fmaf(ga[3], acc[4 * g + 3], bhi(xr[g][1])));
      x1p[g >> 1][(g & 1) * 2] = p0; x1p[g >> 1][(g & 1) * 2 + 1] = p1;
      su = dot2(p0, 0x3F803F80u, su); su = dot2(p1, 0x3F803F80u, su);
      sq = dot2(p0, p0, sq); sq = dot2(p1, p1, sq);
      yp[g >> 1][(g & 1) * 2] = cvtpk(blo(p0) * s2[0], bhi(p0) * s2[1]);
      yp[g >> 1][(g & 1) * 2 + 1] = cvtpk(blo(p1) * s2[2], bhi(p1) * s2[3]);
    }
    su = swsum(su); sq = swsum(sq);
    const u32 tile = mt * 4 + tph * 2 + th, nt = ng * 2 + nh;
    __builtin_amdgcn_raw_buffer_store_b128(yp[0], r_ws, WS_YF + (tile * 28 + nt * 2) * 1024 + lane * 16, 0, 16);
    __builtin_amdgcn_raw_buffer_store_b128(yp[1], r_ws, WS_YF + (tile * 28 + nt * 2 + 1) * 1024 + lane * 16, 0, 16);
    if (hf == 0) __builtin_amdgcn_raw_buffer_store_b64(u32x2{asu(su), asu(sq)}, r_ws, WS_STAT + (tile * 14 + nt) * 256 + l31 * 8, 0, 16);
    WAITV(0);
    if (lane == 0) set_flag(ws, WS_XF + tile * 128 + nt * 4, tag);
    BAR();
    STAMP(15);
#pragma unroll
    for (int i = 3; i < 5; i++) glds7<1>(LB + i * SLOT + w * 7168, lane * 16, s_ws, WS_WGU + j * 196 * 1024 + i * SLOT + w * 7168, 1024, 1024);
  } else {
    const u32 mat = j - 14;
    const auto r_m = RSRC(mat ? wu : wg, 448 * 3584);
    const u32 ch = lane % 14, sub = lane / 14;
    u32x4 wv[28];
#pragma unroll
    for (int i = 0; i < 28; i++) wv[i] = __builtin_amdgcn_raw_buffer_load_b128(r_m, lane < 56 ? (i * 16 + w * 4 + sub) * 3584 + mt * 224 + ch * 16 : 0x7fff0000u, 0, 0);
    if (tid < 224) {
      u32 spins = 0; u64 a, b;
      for (;;) {
        a = __hip_atomic_load((AS1 u64*)(ws + WS_MODG + (896 + tid) * 8), RLX_AGENT);
        b = __hip_atomic_load((AS1 u64*)(ws + WS_MODG + (672 + tid) * 8), RLX_AGENT);
        if ((u32)a == tag && (u32)b == tag) break;
        if (++spins > SPIN_MAX) { set_flag(ws, WS_TMO, tag); break; }
        __builtin_amdgcn_s_sleep(1);
      }
      const u32 cb = (u32)(a >> 32), sb = (u32)(b >> 32), sp = cvtpk(1.f + blo(cb), 1.f + bhi(cb));
      *(AS3 u32x4*)(L + L_P1T + tid * 32) = u32x4{sp & 0xffffu, sp << 16, sb & 0xffffu, sb << 16};
      *(AS3 u32x4*)(L + L_P1T + tid * 32 + 16) = u32x4{sp >> 16, sp & 0xffff0000u, sb >> 16, sb & 0xffff0000u};
    }
    BAR();
    float au[8] = {}, ab[8] = {};
    const u32 subc = sub < 4 ? sub : 0;
#pragma unroll
    for (int i = 0; i < 28; i++) {
      const u32x4 t = LR128(L_P1T + (i * 16 + w * 4 + subc) * 16);
#pragma unroll
      for (int e = 0; e < 4; e++) {
        au[2 * e] = dot2(wv[i][e], t[0], au[2 * e]); au[2 * e + 1] = dot2(wv[i][e], t[1], au[2 * e + 1]);
        ab[2 * e] = dot2(wv[i][e], t[2], ab[2 * e]); ab[2 * e + 1] = dot2(wv[i][e], t[3], ab[2 * e + 1]);
      }
    }
    if (lane < 56) {
      const u32 ro = L_P1R + (w * 4 + sub) * 896 + ch * 32;
      *(AS3 f32x4*)(L + ro) = f32x4{au[0], au[1], au[2], au[3]};
      *(AS3 f32x4*)(L + ro + 16) = f32x4{au[4], au[5], au[6], au[7]};
      *(AS3 f32x4*)(L + ro + 448) = f32x4{ab[0], ab[1], ab[2], ab[3]};
      *(AS3 f32x4*)(L + ro + 464) = f32x4{ab[4], ab[5], ab[6], ab[7]};
    }
    BAR();
    if (tid < 112) {
      float u = 0.f, b = 0.f;
#pragma unroll
      for (int i = 0; i < 16; i++) { u += asf(LR32(L_P1R + i * 896 + tid * 4)); b += asf(LR32(L_P1R + i * 896 + 448 + tid * 4)); }
      const u32 h2 = cvtpk(u, b);
      const u32 l2 = cvtpk(u - blo(h2), b - bhi(h2));
      const u32 uh = h2 & 0xffffu, bh = h2 >> 16, ul = l2 & 0xffffu, bl = l2 >> 16;
      __builtin_amdgcn_raw_buffer_store_b128(u32x4{uh | (uh << 16), ul | (bh << 16), bh | (bl << 16), 0u}, r_ws,
                                             WS_EXF + ((mt * 7 + (tid >> 4)) * 32 + (tid & 15) + 16 * mat) * 16, 0, 16);
    }
    WAITV(0);
    BAR();
    if (tid == 0) set_flag(ws, WS_UF + (mt * 2 + mat) * 4, tag);
    if (w == 3) poll_flags(ws, WS_PF + lane * 4, true, tag), poll_flags(ws, WS_PF + 256 + lane * 4, true, tag), poll_flags(ws, WS_PF + 512 + lane * 4, true, tag), poll_flags(ws, WS_PF + 768 + lane * 4, true, tag);
    BAR();
#pragma unroll
    for (int i = 0; i < 5; i++) glds7<1>(LB + i * SLOT + w * 7168, lane * 16, s_ws, WS_WGU + j * 196 * 1024 + i * SLOT + w * 7168, 1024, 1024);
    poll_flags(ws, WS_OF + (mt * 4) * 128 + (lane & 15) * 4, lane < 16, tag);
    if (bid == 120 && tid == 0) set_flag(ws, WS_EPOCH, tag);
  }

  f32x16 ae[7];
  float r2;
  {
    const u32 tile = mt * 4 + w;
    poll_flags(ws, WS_XF + tile * 128 + (lane & 15) * 4, lane < 14, tag);
    STAMP(16);
    u32x2 st[7];
#pragma unroll
    for (int i = 0; i < 7; i++) st[i] = __builtin_amdgcn_raw_buffer_load_b64(r_ws, l31 * 8 + hf * 7 * 256, WS_STAT + (tile * 14 + i) * 256, 16);
    u32x4 yf[28];
#pragma unroll
    for (int s = 0; s < 28; s++) yf[s] = __builtin_amdgcn_raw_buffer_load_b128(r_ws, lane * 16, WS_YF + (tile * 28 + s) * 1024, 16);
    float su = 0.f, sq = 0.f;
#pragma unroll
    for (int i = 0; i < 7; i++) { su += asf(st[i][0]); sq += asf(st[i][1]); }
    const float m = swsum(su) * (1.f / 448.f), var = fmaxf(swsum(sq) * (1.f / 448.f) - m * m, 0.f) + 1e-5f;
    const float rr = __builtin_amdgcn_rsqf(var), ir = var * rr;
    r2 = rr * rr;
    if (hf == 0) *(AS3 float*)(L + L_R2 + (w * 32 + l31) * 4) = r2;
    u32x4 bx;
    {
      const u32 h2 = cvtpk(-m, ir), l2 = cvtpk(-m - blo(h2), ir - bhi(h2));
      const u32 mh = h2 & 0xffffu, ih = h2 >> 16, ml = l2 & 0xffffu, il = l2 >> 16;
      bx = hf ? u32x4{0, 0, 0, 0} : u32x4{mh | (ml << 16), mh | (ih << 16), il | (ih << 16), 0u};
    }
#pragma unroll
    for (int t = 0; t < 7; t++) ae[t] = f32x16{};
    WAITV(0);
    BAR();
    STAMP(17);
#pragma unroll
    for (int c = 0; c < 7; c++) {
      if (c == 1 || c == 2) { BAR(); glds7<1>(LB + (c - 1) * SLOT + w * 7168, lane * 16, s_ws, WS_WGU + j * 196 * 1024 + (c + 4) * SLOT + w * 7168, 1024, 1024); }
      if (c == 5) { WAITV(7); BAR(); }
      if (c == 6) { WAITV(0); BAR(); }
#pragma unroll
      for (int k = 0; k < 4; k++)
#pragma unroll
        for (int t = 0; t < 7; t++) ae[t] = mfma(LR128((c % 5) * SLOT + (k * 7 + t) * 1024 + lane * 16), yf[c * 4 + k], ae[t]);
    }
    poll_flags(ws, WS_UF + (j * 2 + (lane & 1)) * 4, lane < 2, tag);
#pragma unroll
    for (int t = 0; t < 7; t++)
      ae[t] = mfma(__builtin_amdgcn_raw_buffer_load_b128(r_ws, hf ? 0x7fff0000u : l31 * 16, WS_EXF + (j * 7 + t) * 512, 16), bx, ae[t]);
    STAMP(18);
#pragma unroll
    for (int t = 0; t < 7; t++) {
      const u32x4 hv = u32x4{cvtpk(ae[t][0] * ae[t][8], ae[t][1] * ae[t][9]), cvtpk(ae[t][2] * ae[t][10], ae[t][3] * ae[t][11]),
                             cvtpk(ae[t][4] * ae[t][12], ae[t][5] * ae[t][13]), cvtpk(ae[t][6] * ae[t][14], ae[t][7] * ae[t][15])};
      __builtin_amdgcn_raw_buffer_store_b128(hv, r_ws, WS_HH + (tile * 112 + j * 7 + t) * 1024 + lane * 16, 0, 16);
    }
    WAITV(0);
    if (lane == 0) set_flag(ws, WS_HF + tile * 128 + j * 4, tag);
  }
  STAMP(19);

  if (dwg) {
    const u32 kh = th, nt = ng * 2 + nh;
    u32x4 wdf[56];
#pragma unroll
    for (int s = 0; s < 56; s++) wdf[s] = __builtin_amdgcn_raw_buffer_load_b128(r_ws, lane * 16, WS_WD + (nt * 112 + kh * 56 + s) * 1024, 16);
    BAR();
    const u32 ltile = mt * 4 + tph * 2 + nh;
    poll_flags(ws, WS_HF + ltile * 128 + (kh * 8 + (lane & 7)) * 4, lane < 8, tag);
    STAMP(20);
    const u32 ldst = LB + (kh * 2 + nh) * 7168, lsrc = WS_HH + (ltile * 112 + kh * 56) * 1024;
#pragma unroll
    for (int c = 0; c < 5; c++) glds7<1>(ldst + c * SLOT, lane * 16, s_ws, lsrc + c * 7168, 1024, 1024);
    f32x16 af[2] = {};
#pragma unroll
    for (int c = 0; c < 8; c++) {
      if (c == 0) { WAITV(28); } else if (c <= 4) { WAITV(21); } else if (c == 5) { WAITV(14); } else if (c == 6) { WAITV(7); } else { WAITV(0); }
      BAR();
      if (c >= 1 && c <= 3) glds7<1>(ldst + (c - 1) * SLOT, lane * 16, s_ws, lsrc + (c + 4) * 7168, 1024, 1024);
#pragma unroll
      for (int k = 0; k < 7; k++)
#pragma unroll
        for (int tt = 0; tt < 2; tt++) af[tt] = mfma(wdf[c * 7 + k], LR128((c % 5) * SLOT + (kh * 2 + tt) * 7168 + k * 1024 + lane * 16), af[tt]);
    }
    STAMP(21);
    BAR();
    {
      const f32x16& snd = kh ? af[0] : af[1];
      const u32 ad = (w ^ 2) * 4096 + lane * 16;
#pragma unroll
      for (int g = 0; g < 4; g++) *(AS3 f32x4*)(L + ad + g * 1024) = f32x4{snd[4 * g], snd[4 * g + 1], snd[4 * g + 2], snd[4 * g + 3]};
    }
    BAR();
    const float rr2 = *(AS3 float*)(L + L_R2 + (tph * 64 + th * 32 + l31) * 4);
    u32 po[8];
#pragma unroll
    for (int g = 0; g < 4; g++) {
      const f32x4 ot = *(AS3 f32x4*)(L + w * 4096 + lane * 16 + g * 1024);
      const f32x4 gb = *(AS3 f32x4*)(L + L_GB + (nh * 32 + g * 8 + 4 * hf) * 4);
      float v[4];
#pragma unroll
      for (int e = 0; e < 4; e++) v[e] = ((kh ? af[1][4 * g + e] : af[0][4 * g + e]) + ot[e]) * rr2 * gb[e];
      const u32 p0 = x1p[g >> 1][(g & 1) * 2], p1 = x1p[g >> 1][(g & 1) * 2 + 1];
      po[2 * g] = cvtpk(blo(p0) + v[0], bhi(p0) + v[1]);
      po[2 * g + 1] = cvtpk(blo(p1) + v[2], bhi(p1) + v[3]);
    }
    const auto r_o = RSRC(out, 2048 * 896);
    const u32 oo = (mt * 128 + tph * 64 + th * 32 + l31) * 896 + (ng * 64 + nh * 32) * 2 + hf * 16;
#pragma unroll
    for (int g2 = 0; g2 < 2; g2++) {
      auto s0 = __builtin_amdgcn_permlane32_swap(po[4 * g2], po[4 * g2 + 2], false, false);
      auto s1 = __builtin_amdgcn_permlane32_swap(po[4 * g2 + 1], po[4 * g2 + 3], false, false);
      __builtin_amdgcn_raw_buffer_store_b128(u32x4{s0[0], s1[0], s0[1], s1[1]}, r_o, oo + g2 * 32, 0, 0);
    }
  }
  STAMP(22);
}

extern "C" void kernel_launch(void* const* d_in, const int* in_sizes, int n_in, void* d_out, int out_size, void* d_ws, size_t ws_size, hipStream_t stream) {
  (void)in_sizes; (void)n_in; (void)out_size; (void)ws_size;
  hipFuncSetAttribute((const void*)fused_block, hipFuncAttributeMaxDynamicSharedMemorySize, LDS_BYTES);
  fused_block<<<256, 256, LDS_BYTES, stream>>>((const u16*)d_in[0], (const u16*)d_in[1], (const u16*)d_in[2], (const u16*)d_in[3], (const u16*)d_in[5], (const u16*)d_in[6],
                                               (const u16*)d_in[7], (const u16*)d_in[8], (const u16*)d_in[9], (u16*)d_out, (char*)d_ws);
}
```

```cpp
#include <hip/hip_runtime.h>
#include <stdint.h>
#include <utility>

typedef unsigned u32;
typedef unsigned short u16;
typedef unsigned long long u64;
typedef u32 u32x4 __attribute__((ext_vector_type(4), may_alias));
typedef u32 u32x2 __attribute__((ext_vector_type(2), may_alias));
typedef float f32x16 __attribute__((ext_vector_type(16)));
typedef float f32x4 __attribute__((ext_vector_type(4), may_alias));
typedef float f32x2 __attribute__((ext_vector_type(2), may_alias));
typedef __bf16 bf16x8 __attribute__((ext_vector_type(8)));
typedef __bf16 bf16x2 __attribute__((ext_vector_type(2)));
typedef short s16x4 __attribute__((ext_vector_type(4)));
#define DI __device__ __forceinline__
#define AS3 __attribute__((address_space(3)))
#define AS1 __attribute__((address_space(1)))
#define RLX_AGENT __ATOMIC_RELAXED, __HIP_MEMORY_SCOPE_AGENT

constexpr u32 WS_EPOCH = 0, WS_TMO = 256;
constexpr u32 WS_MODG = 4096;
constexpr u32 WS_KVF = 16384;
constexpr u32 WS_OF = 20480;
constexpr u32 WS_XF = 28672;
constexpr u32 WS_HF = 36864;
constexpr u32 WS_PF = 45056;
constexpr u32 WS_UF = 46080;
constexpr u32 WS_PD = 46336;
constexpr u32 WS_KVP = 65536;
constexpr u32 WS_OFR = WS_KVP + (1u << 20);
constexpr u32 WS_YF = WS_OFR + (2u << 20);
constexpr u32 WS_STAT = WS_YF + 64 * 28 * 1024;
constexpr u32 WS_HH = WS_STAT + 64 * 14 * 256;
constexpr u32 WS_WGU = WS_HH + 64 * 112 * 1024;
constexpr u32 WS_WD = WS_WGU + 3136 * 1024;
constexpr u32 WS_EXF = WS_WD + 1568 * 1024;
constexpr u32 WS_END = WS_EXF + 112 * 512;
constexpr u32 WS_DBG = 24u << 20;

constexpr u32 L_XR = 0, XRW = 28672;
constexpr u32 L_W1 = 114688;
constexpr u32 L_PHI = 151552;
constexpr u32 L_TAB = 153600;
constexpr u32 L_RED = 160768;
constexpr u32 L_PHK = 57344, L_VIM = 73728, L_KVS = 81920;
constexpr u32 RING = 150;
constexpr u32 L_WO = 94 * 1024;
constexpr u32 L_DEX = 134 * 1024;
constexpr u32 L_XRES = 153600;
constexpr u32 L_GA = 161792, L_S2 = 162048, L_GB = 162304, L_R2 = 162560;
constexpr u32 L_P1T = 0, L_P1R = 8192;
constexpr u32 L_FEX = 128 * 1024;
constexpr u32 LDS_BYTES = 163840;

constexpr u32 SPIN_MAX = 100000;
constexpr u32 CODE_KB = 47;
constexpr int PRE_A = 80;

template <class F, int... Is> DI void sfor_(F&& f, std::integer_sequence<int, Is...>) { (f(std::integral_constant<int, Is>{}), ...); }
template <int N, class F> DI void sfor(F&& f) { sfor_(f, std::make_integer_sequence<int, N>{}); }
#define LAM(i) [&](auto i##_) __attribute__((always_inline))

DI f32x16 mfma(u32x4 a, u32x4 b, f32x16 c) {
  return __builtin_amdgcn_mfma_f32_32x32x16_bf16(__builtin_bit_cast(bf16x8, a), __builtin_bit_cast(bf16x8, b), c, 0, 0, 0);
}
DI float asf(u32 x) { return __builtin_bit_cast(float, x); }
DI u32 asu(float x) { return __builtin_bit_cast(u32, x); }
DI u32 cvtpk(float lo, float hi) { f32x2 v = {lo, hi}; return __builtin_bit_cast(u32, __builtin_convertvector(v, bf16x2)); }
DI float dot2(u32 a, u32 b, float c) { return __builtin_amdgcn_fdot2_f32_bf16(__builtin_bit_cast(bf16x2, a), __builtin_bit_cast(bf16x2, b), c, false); }
DI float blo(u32 d) { return asf(d << 16); }
DI float bhi(u32 d) { return asf(d & 0xffff0000u); }
DI u32x4 pack8(const f32x16& c, int o) { return u32x4{cvtpk(c[o], c[o + 1]), cvtpk(c[o + 2], c[o + 3]), cvtpk(c[o + 4], c[o + 5]), cvtpk(c[o + 6], c[o + 7])}; }
DI u32x4 cat(u32x2 a, u32x2 b) { return u32x4{a[0], a[1], b[0], b[1]}; }
DI float swsum(float v) { auto r = __builtin_amdgcn_permlane32_swap(asu(v), asu(v), false, false); return asf(r[0]) + asf(r[1]); }
DI float swmax(float v) { auto r = __builtin_amdgcn_permlane32_swap(asu(v), asu(v), false, false); return fmaxf(asf(r[0]), asf(r[1])); }
DI float swmin(float v) { auto r = __builtin_amdgcn_permlane32_swap(asu(v), asu(v), false, false); return fminf(asf(r[0]), asf(r[1])); }
DI u32x4 mksrd(const void* p, u32 bytes) { u64 a = (u64)p; return u32x4{(u32)a, (u32)(a >> 32) & 0xffffu, bytes, 0x00020000u}; }
#define RSRC(p, bytes) __builtin_amdgcn_make_buffer_rsrc((void*)(p), 0, (bytes), 0x00020000)
#define WAITV(n) asm volatile("s_waitcnt vmcnt(%0)" ::"n"(n) : "memory")
#define BAR() asm volatile("s_waitcnt lgkmcnt(0)\n\ts_barrier" ::: "memory")
#define RAWBAR() asm volatile("s_barrier" ::: "memory")
#define GAP() __builtin_amdgcn_sched_barrier(0)
#define VWAITN(n) __builtin_amdgcn_s_waitcnt(((n) & 15) | (((n) >> 4) << 14) | 0x0F70)
#define VWAIT0() VWAITN(0)
constexpr u32 OOB = 0x7fff0000u;
DI void glds(u32 m0, u32 voff, u32x4 srd, u32 soff) {
  asm volatile("s_mov_b32 m0, %0\n\ts_nop 0\n\tbuffer_load_dwordx4 %1, %2, %3 offen lds" ::"s"(m0), "v"(voff), "s"(srd), "s"(soff) : "memory", "m0");
}
DI void glds1(u32 m0, u32 voff, u32x4 srd, u32 soff) {
  asm volatile("s_mov_b32 m0, %0\n\ts_nop 0\n\tbuffer_load_dwordx4 %1, %2, %3 offen sc1 lds" ::"s"(m0), "v"(voff), "s"(srd), "s"(soff) : "memory", "m0");
}
#define GN_STEP(SC) "s_add_u32 m0, m0, %[dm]\n\ts_add_u32 %[so], %[so], %[dg]\n\tbuffer_load_dwordx4 %[v], %[srd], %[so] offen " SC "lds\n\t"
#define GN_HEAD(SC) "s_mov_b32 m0, %[m]\n\ts_nop 0\n\tbuffer_load_dwordx4 %[v], %[srd], %[so] offen " SC "lds\n\t"
#define GN_ARGS : [so] "+s"(soff) : [m] "s"(m0), [v] "v"(voff), [srd] "s"(srd), [dm] "s"(dm), [dg] "s"(dg) : "memory", "scc", "m0"
template <int SC1> DI void glds7(u32 m0, u32 voff, u32x4 srd, u32 soff, u32 dm, u32 dg) {
  if constexpr (SC1) asm volatile(GN_HEAD("sc1 ") GN_STEP("sc1 ") GN_STEP("sc1 ") GN_STEP("sc1 ") GN_STEP("sc1 ") GN_STEP("sc1 ") GN_STEP("sc1 ") GN_ARGS);
  else asm volatile(GN_HEAD("") GN_STEP("") GN_STEP("") GN_STEP("") GN_STEP("") GN_STEP("") GN_STEP("") GN_ARGS);
}
template <int SC1> DI void glds4(u32 m0, u32 voff, u32x4 srd, u32 soff, u32 dm, u32 dg) {
  if constexpr (SC1) asm volatile(GN_HEAD("sc1 ") GN_STEP("sc1 ") GN_STEP("sc1 ") GN_STEP("sc1 ") GN_ARGS);
  else asm volatile(GN_HEAD("") GN_STEP("") GN_STEP("") GN_STEP("") GN_ARGS);
}

DI void glds4x(u32 m0, u32 v0, u32 v1, u32x4 srd, u32 soff) {
  asm volatile("s_mov_b32 m0, %[m]\n\ts_nop 0\n\tbuffer_load_dwordx4 %[v0], %[srd], %[so] offen lds\n\t"
               "s_add_u32 m0, m0, 0x400\n\ts_add_u32 %[so], %[so], 0x1c00\n\tbuffer_load_dwordx4 %[v1], %[srd], %[so] offen lds\n\t"
               "s_add_u32 m0, m0, 0x400\n\ts_add_u32 %[so], %[so], 0x1c00\n\tbuffer_load_dwordx4 %[v0], %[srd], %[so] offen lds\n\t"
               "s_add_u32 m0, m0, 0x400\n\ts_add_u32 %[so], %[so], 0x1c00\n\tbuffer_load_dwordx4 %[v1], %[srd], %[so] offen lds"
               : [so] "+s"(soff) : [m] "s"(m0), [v0] "v"(v0), [v1] "v"(v1), [srd] "s"(srd) : "memory", "scc", "m0");
}

#define STAMP(i)
#define STAMPC(i)
#define STAMP_FLUSH()

DI void poll_flags(const char* ws, u32 off, bool act, u32 tag) {
  u32 spins = 0;
  for (;;) {
    u32 f = act ? __hip_atomic_load((AS1 u32*)(ws + off), RLX_AGENT) : tag;
    if (__all(f == tag)) break;
    if (++spins > SPIN_MAX) { __hip_atomic_store((AS1 u32*)(ws + WS_TMO), tag, RLX_AGENT); break; }
    __builtin_amdgcn_s_sleep(1);
  }
  asm volatile("" ::: "memory");
}
DI void set_flag(char* ws, u32 off, u32 tag) { __hip_atomic_store((AS1 u32*)(ws + off), tag, RLX_AGENT); }
DI u32 get_granule(char* ws, u32 off, u32 tag) {
  u32 spins = 0; u64 a;
  for (;;) {
    a = __hip_atomic_load((AS1 u64*)(ws + off), RLX_AGENT);
    if ((u32)a == tag) break;
    if (++spins > SPIN_MAX) { set_flag(ws, WS_TMO, tag); break; }
    __builtin_amdgcn_s_sleep(1);
  }
  return (u32)(a >> 32);
}

DI void hedgehog(const f32x16& p, u32 hf, u32x4 (&fr)[4]) {
  constexpr float LG = 1.4426950408889634f;
  const float NINF = -__builtin_inff();
  float pp[16], pn[16];
#pragma unroll
  for (int r = 0; r < 16; r++) { pp[r] = p[r]; pn[r] = p[r]; }
#pragma unroll
  for (int r = 12; r < 16; r++) { pp[r] = hf ? NINF : p[r]; pn[r] = hf ? -NINF : p[r]; }
  float mx = pp[0], mn = pn[0];
#pragma unroll
  for (int r = 1; r < 16; r++) { mx = fmaxf(mx, pp[r]); mn = fminf(mn, pn[r]); }
  mx = swmax(mx); mn = swmin(mn);
  const float a = -mx * LG, b = mn * LG;
  float s0 = 0.f, s1 = 0.f;
#pragma unroll
  for (int r = 0; r < 16; r++) {
    pp[r] = __builtin_amdgcn_exp2f(__builtin_fmaf(pp[r], LG, a)); s0 += pp[r];
    pn[r] = __builtin_amdgcn_exp2f(__builtin_fmaf(pn[r], -LG, b)); s1 += pn[r];
  }
  s0 = __builtin_amdgcn_rcpf(swsum(s0)); s1 = __builtin_amdgcn_rcpf(swsum(s1));
#pragma unroll
  for (int k = 0; k < 2; k++) {
    fr[k] = u32x4{cvtpk(pp[8 * k] * s0, pp[8 * k + 1] * s0), cvtpk(pp[8 * k + 2] * s0, pp[8 * k + 3] * s0), cvtpk(pp[8 * k + 4] * s0, pp[8 * k + 5] * s0), cvtpk(pp[8 * k + 6] * s0, pp[8 * k + 7] * s0)};
    fr[2 + k] = u32x4{cvtpk(pn[8 * k] * s1, pn[8 * k + 1] * s1), cvtpk(pn[8 * k + 2] * s1, pn[8 * k + 3] * s1), cvtpk(pn[8 * k + 4] * s1, pn[8 * k + 5] * s1), cvtpk(pn[8 * k + 6] * s1, pn[8 * k + 7] * s1)};
  }
}
DI u32x4 pack8s(const f32x16& c, int o, float s) {
  return u32x4{cvtpk(c[o] * s, c[o + 1] * s), cvtpk(c[o + 2] * s, c[o + 3] * s), cvtpk(c[o + 4] * s, c[o + 5] * s), cvtpk(c[o + 6] * s, c[o + 7] * s)};
}
DI u32x4 ln_operand(float m, float ir, u32 hf) {
  const u32 h2 = cvtpk(-m, ir), l2 = cvtpk(-m - blo(h2), ir - bhi(h2));
  return hf ? u32x4{0, 0, 0, 0} : u32x4{(h2 & 0xffffu) | (l2 << 16), (h2 >> 16) | (l2 & 0xffff0000u), 0u, 0u};
}

__global__ __launch_bounds__(256, 1) void fused_block(const u16* __restrict__ x, const u16* __restrict__ wqkv, const u16* __restrict__ wphi, const u16* __restrict__ wo,
                                                      const u16* __restrict__ wg, const u16* __restrict__ wu, const u16* __restrict__ wd, const u16* __restrict__ cvec,
                                                      const u16* __restrict__ wmod, u16* __restrict__ out, char* __restrict__ ws) {
  extern __shared__ __attribute__((aligned(16))) char lds_[];
  AS3 char* L = (AS3 char*)lds_;
  const u32 LB = (u32)(size_t)L;
#define LR128(off) (*(AS3 u32x4*)(L + (off)))
#define LR64(off) (*(AS3 u32x2*)(L + (off)))
#define LR32(off) (*(AS3 u32*)(L + (off)))
#define TRR(off) __builtin_bit_cast(u32x2, __builtin_amdgcn_ds_read_tr16_b64_v4i16((AS3 s16x4*)(L + (off))))
#define BASES(name, expr) u32 name[3] = {(expr), (expr) + 65536, (expr) + 131072}; asm volatile("" : "+v"(name[0]), "+v"(name[1]), "+v"(name[2]))
#define AT(name, off) (name[(off) >> 16] + ((off) & 65535))
  const u32 w = __builtin_amdgcn_readfirstlane(threadIdx.x >> 6);
  const u32 bid = blockIdx.x;
  const u32 xh = bid & 1, mt = ((bid & 7) >> 1) * 4 + ((bid >> 3) & 3), jj = bid >> 5;
  const u32 j = xh * 8 + (((xh ? 0x76425310u : 0x75316420u) >> (jj * 4)) & 7);
  u32 lane, tid, l31, hf, tq, tp_, tblk;
#define RELANE() do { lane = __builtin_amdgcn_mbcnt_hi(~0u, __builtin_amdgcn_mbcnt_lo(~0u, 0u)); asm volatile("" : "+v"(lane)); __builtin_assume(lane < 64); \
    tid = w * 64 + lane; l31 = lane & 31; hf = lane >> 5; tq = (lane & 15) >> 2; tp_ = lane & 3; tblk = (lane >> 4) & 1; } while (0)
  RELANE();
  const u32x4 s_ws = mksrd(ws, WS_END);
  const auto r_ws = RSRC(ws, WS_END);
  STAMP(0);

  const u32 gg = bid < 32 ? bid : (bid - 128u < 52u ? bid - 96 : 255);
  const bool gemv = gg < 84;
  u32 vep, vc;
  u32x4 wm[7];
  {
    const u32x4 s_c = mksrd(cvec, 896), s_wm = mksrd(wmod, 448 * 5376);
    const u32 vo = (w * 112 + (lane >> 2)) * 5376 + gg * 64 + (lane & 3) * 16;
    asm volatile(
        "buffer_load_dword %[ep], off, %[sws], 0 sc1\n\t"
        "s_cmp_ge_u32 %[g], 84\n\t"
        "s_cbranch_scc1 .Lnogemv%=\n\t"
        "buffer_load_dword %[c], %[vc], %[sc], 0 offen\n\t"
        "buffer_load_dwordx4 %[w0], %[vo], %[sm], 0 offen\n\t"
        "buffer_load_dwordx4 %[w1], %[vo], %[sm], %[o1] offen\n\t"
        "buffer_load_dwordx4 %[w2], %[vo], %[sm], %[o2] offen\n\t"
        "buffer_load_dwordx4 %[w3], %[vo], %[sm], %[o3] offen\n\t"
        "buffer_load_dwordx4 %[w4], %[vo], %[sm], %[o4] offen\n\t"
        "buffer_load_dwordx4 %[w5], %[vo], %[sm], %[o5] offen\n\t"
        "buffer_load_dwordx4 %[w6], %[vo], %[sm], %[o6] offen\n"
        ".Lnogemv%=:"
        : [ep] "=&v"(vep), [c] "=&v"(vc), [w0] "=&v"(wm[0]), [w1] "=&v"(wm[1]), [w2] "=&v"(wm[2]), [w3] "=&v"(wm[3]), [w4] "=&v"(wm[4]), [w5] "=&v"(wm[5]), [w6] "=&v"(wm[6])
        : [g] "s"(gg), [vc] "v"(w * 224 + lane * 4), [vo] "v"(vo), [sc] "s"(s_c), [sm] "s"(s_wm), [sws] "s"(s_ws), [o1] "s"(86016u), [o2] "s"(2 * 86016u), [o3] "s"(3 * 86016u),
          [o4] "s"(4 * 86016u), [o5] "s"(5 * 86016u), [o6] "s"(6 * 86016u)
        : "memory", "scc");
  }
  const u32x4 s_x0 = mksrd(x, 2048 * 896);
  const u32 xve = (lane >> 3) * 896 + (((lane & 7) ^ (lane >> 4)) << 4), xvo = xve ^ 64;
  const u32 xso = (mt * 128 + w * 32) * 896, xm = LB + L_XR + w * XRW;
#define X_LD(b) glds4x(xm + (b) * 4096, xve, xvo, s_x0, xso + (b) * 128)
  X_LD(0); X_LD(1);
  {
    const u64 pc = __builtin_amdgcn_s_getpc();
    const u32 piece = ((bid >> 3) * 4 + w) % CODE_KB;
    asm volatile("s_mov_b32 m0, %0\n\ts_nop 0\n\tglobal_load_lds_dwordx4 %1, off" ::"s"(LB + LDS_BYTES - 1024), "v"((pc & ~(u64)1023) + piece * 1024 + lane * 16) : "memory", "m0");
  }
  STAMP(1);
  if (gemv) {
    asm volatile("s_waitcnt vmcnt(9)" : "+v"(vep), "+v"(vc), "+v"(wm[0]), "+v"(wm[1]), "+v"(wm[2]), "+v"(wm[3]), "+v"(wm[4]), "+v"(wm[5]), "+v"(wm[6])::"memory");
    const u32 tag0 = __builtin_amdgcn_readfirstlane(vep) * 5u + 0x3C6EF35Fu;
    const float c0 = blo(vc), c1 = bhi(vc);
    const float si0 = c0 * __builtin_amdgcn_rcpf(1.f + __builtin_amdgcn_exp2f(-1.4426950408889634f * c0));
    const float si1 = c1 * __builtin_amdgcn_rcpf(1.f + __builtin_amdgcn_exp2f(-1.4426950408889634f * c1));
    const u32 sp = cvtpk(si0, si1);
    float a[8] = {};
#pragma unroll
    for (int i = 0; i < 7; i++) {
      const u32 t = (u32)__builtin_amdgcn_ds_bpermute((int)((i * 8 + (lane >> 3)) * 4), (int)sp);
      const u32 sl = (lane & 4) ? t >> 16 : t & 0xffffu, sh = sl << 16;
#pragma unroll
      for (int d = 0; d < 4; d++) { a[2 * d] = dot2(wm[i][d], sl, a[2 * d]); a[2 * d + 1] = dot2(wm[i][d], sh, a[2 * d + 1]); }
    }
    float b[4], c[2];
#pragma unroll
    for (int i = 0; i < 4; i++) { auto r = __builtin_amdgcn_permlane32_swap(asu(a[i]), asu(a[i + 4]), false, false); b[i] = asf(r[0]) + asf(r[1]); }
#pragma unroll
    for (int i = 0; i < 2; i++) {
      auto r = __builtin_amdgcn_permlane16_swap(asu(b[i]), asu(b[i + 2]), false, false);
      float v = asf(r[0]) + asf(r[1]);
      v += asf(__builtin_amdgcn_update_dpp(0, asu(v), 0x128, 0xf, 0xf, true));
      v += asf(__builtin_amdgcn_update_dpp(0, asu(v), 0x124, 0xf, 0xf, true));
      c[i] = v;
    }
    const u32 p = lane >> 4;
    if ((lane & 12) == 0) *(AS3 f32x2*)(L + L_RED + w * 128 + ((lane & 3) * 4 + (p & 1) + 2 * (p >> 1)) * 8) = f32x2{c[0], c[1]};
    BAR();
    if (tid < 16) {
      f32x2 t = *(AS3 f32x2*)(L + L_RED + tid * 8);
#pragma unroll
      for (int i = 1; i < 4; i++) { f32x2 u = *(AS3 f32x2*)(L + L_RED + i * 128 + tid * 8); t += u; }
      const u64 g = ((u64)cvtpk(t[0], t[1]) << 32) | tag0;
      __hip_atomic_store((AS1 u64*)(ws + WS_MODG + (gg * 16 + tid) * 8), g, RLX_AGENT);
    }
  }
  STAMP(2);
  X_LD(2); X_LD(3); X_LD(4); X_LD(5); X_LD(6);
  const u32x4 s_w = mksrd(wqkv, 448 * 2688);
  const u32 winb = j * 56 - (j & 1) * 8;
  u32 vw;
  {
    const u32 ci = w * 64 + lane, row = (ci * 43691) >> 19, ch = ci - row * 12;
    vw = row * 2688 + (ch >> 2) * 896 + (ch & 3) * 16;
  }
#define W_LD(g) glds(LB + ((g) < 12 ? L_W1 + (g) * 3072 : L_XR + (((g) - 12) >> 2) * XRW + (((g) - 12) & 3) * 3072) + w * 1024, vw, s_w, (g) * 43008 + winb)
  u32x2 gsa[4], gca[4];
  const u32 gofs = WS_MODG + lane * 8;
#define G_LD() asm volatile("buffer_load_dwordx2 %0, %8, %10, 0 offen sc1\n\tbuffer_load_dwordx2 %1, %8, %10, 0 offen offset:512 sc1\n\t" \
                            "buffer_load_dwordx2 %2, %8, %10, 0 offen offset:1024 sc1\n\tbuffer_load_dwordx2 %3, %9, %10, 0 offen offset:1536 sc1\n\t" \
                            "buffer_load_dwordx2 %4, %8, %10, 0 offen offset:1792 sc1\n\tbuffer_load_dwordx2 %5, %8, %10, 0 offen offset:2304 sc1\n\t" \
                            "buffer_load_dwordx2 %6, %8, %10, 0 offen offset:2816 sc1\n\tbuffer_load_dwordx2 %7, %9, %10, 0 offen offset:3328 sc1" \
                            : "=&v"(gsa[0]), "=&v"(gsa[1]), "=&v"(gsa[2]), "=&v"(gsa[3]), "=&v"(gca[0]), "=&v"(gca[1]), "=&v"(gca[2]), "=&v"(gca[3]) \
                            : "v"(gofs), "v"(gofs - hf * 256), "s"(s_ws) : "memory")
#define WAITV2(n012, n3) asm volatile("s_cmp_eq_u32 %0, 3\n\ts_cbranch_scc1 .Lw3%=\n\ts_waitcnt vmcnt(%1)\n\ts_branch .Lwe%=\n.Lw3%=:\n\ts_waitcnt vmcnt(%2)\n.Lwe%=:" \
                                      ::"s"(w), "n"(n012), "n"(n3) : "memory", "scc")

  u32x4 xf[28];
  float rs;
  u32x4 bx1;
  {
    const u32 sw = (l31 >> 1) & 7, b0 = L_XR + w * XRW + (l31 >> 3) * 1024 + (l31 & 7) * 128;
    u32 xa[4];
#pragma unroll
    for (int m = 0; m < 4; m++) xa[m] = b0 + (((2 * m + hf) ^ sw) << 4);
    float su[2] = {0.f, 0.f}, sq[2] = {0.f, 0.f};
    sfor<7>(LAM(b) {
      constexpr int b = b_.value;
      constexpr int nx = b == 0 ? 25 : b == 1 ? 21 : (6 - b) * 4;
      WAITV2(nx + 2 * b, nx + (b == 0 ? 0 : b < 6 ? 2 : 10));
      if constexpr (b == 0) asm volatile("" : "+v"(vep));
      if constexpr (b == 0) { STAMP(3); }
#pragma unroll
      for (int m = 0; m < 4; m++) xf[b * 4 + m] = LR128(xa[m] + b * 4096);
      if constexpr (b < 6) {
        if (w < 3) { W_LD(2 * b); W_LD(2 * b + 1); }
        else if constexpr (b == 0) {
          const u32x4 s_ph = mksrd(wphi, 16 * 1568);
          glds(LB + L_PHI, j * 1568 + lane * 16, s_ph, 0);
          glds(LB + L_PHI + 1024, j * 1568 + lane * 16 + 1024, s_ph, 0);
        } else if constexpr (b == 5) G_LD();
      }
      GAP();
#pragma unroll
      for (int m = 0; m < 4; m++)
#pragma unroll
        for (int i = 0; i < 4; i++) { su[i & 1] = dot2(xf[b * 4 + m][i], 0x3F803F80u, su[i & 1]); sq[i & 1] = dot2(xf[b * 4 + m][i], xf[b * 4 + m][i], sq[i & 1]); }
      GAP();
    });
    STAMP(4);
    const float m = swsum(su[0] + su[1]) * (1.f / 448.f), e2 = swsum(sq[0] + sq[1]) * (1.f / 448.f);
    const float var = fmaxf(e2 - m * m, 0.f) + 1e-5f;
    rs = __builtin_amdgcn_rsqf(var);
    bx1 = ln_operand(m, var * rs, hf);
  }
  const u32 tag = __builtin_amdgcn_readfirstlane(vep) * 5u + 0x3C6EF35Fu;
  STAMP(5);
  if (w == 3) {
    asm volatile("s_waitcnt vmcnt(0)" : "+v"(gsa[0]), "+v"(gsa[1]), "+v"(gsa[2]), "+v"(gsa[3]), "+v"(gca[0]), "+v"(gca[1]), "+v"(gca[2]), "+v"(gca[3])::"memory");
    u32 spins = 0;
    for (;;) {
      bool ok = true;
#pragma unroll
      for (int q = 0; q < 4; q++) ok = ok && gsa[q][0] == tag && gca[q][0] == tag;
      if (__all(ok)) break;
      if (++spins > SPIN_MAX) { set_flag(ws, WS_TMO, tag); break; }
      G_LD();
      asm volatile("s_waitcnt vmcnt(0)" : "+v"(gsa[0]), "+v"(gsa[1]), "+v"(gsa[2]), "+v"(gsa[3]), "+v"(gca[0]), "+v"(gca[1]), "+v"(gca[2]), "+v"(gca[3])::"memory");
    }
#pragma unroll
    for (int q = 0; q < 4; q++) {
      if (q < 3 || lane < 32) {
        const u32 sa = gsa[q][1], sp = cvtpk(1.f + blo(gca[q][1]), 1.f + bhi(gca[q][1]));
        const u32 ad = L_TAB + (q * 64 + lane) * 32;
        *(AS3 u32x4*)(L + ad) = u32x4{(sp & 0xffffu) * 0x10001u, (sa & 0xffffu) * 0x10001u, 0, 0};
        *(AS3 u32x4*)(L + ad + 16) = u32x4{(sp >> 16) * 0x10001u, (sa >> 16) * 0x10001u, 0, 0};
      }
    }
    BAR();
  } else {
    WAITV(8);
    BAR();
  }
  STAMP(6); STAMPC(40);

  RELANE();
  const u32 njob = bid >= 176 ? 2 : 1;
  const bool gu0 = bid < 224;
  const u32 pv1 = lane < 56 ? lane * 16 : OOB;
  const u32 vdm = w < 3 ? vw : (gu0 ? (lane < 56 ? (lane >= 28 ? 3584 - 448 : 0) + lane * 16 : OOB) : pv1);
  u32x4 sdm = w < 3 ? s_w : (gu0 ? mksrd(wg, 448 * 3584) : mksrd(wd, 1792 * 896));
  u32 mdm = LB + L_XR + (w < 3 ? w * 1024 : 12288), odm = w < 3 ? 12 * 43008 + winb : (gu0 ? (bid >> 3) * 16 * 3584 + (bid & 7) * 448 : (bid - 224) * 16 * 896);
  const u32 mds = w < 3 ? 3072 : 1024, mdx = w < 3 ? XRW - 3 * 3072 : 1024;
  const u32 ods = w < 3 ? 43008 : (gu0 ? 7168 : 896), odx = w < 3 ? 43008 : (gu0 ? 0u - 7 * 7168 : 896);
  const u64 ddm = (w == 3 && gu0) ? (u64)wu - (u64)wg : 0;
#define XPD(x) x
#define B_REQ(q) do { XPD(glds(mdm, vdm, sdm, odm)); mdm += ((q) & 3) == 3 ? mdx : mds; odm += (q) == 7 ? odx : ods; \
    if constexpr ((q) == 7) { const u64 a_ = (((u64)sdm[1] << 32) | sdm[0]) + ddm; sdm[0] = (u32)a_; sdm[1] = (u32)(a_ >> 32); } } while (0)
#define B_WAIT(n) asm volatile("s_cmp_eq_u32 %0, 3\n\ts_cbranch_scc1 .Lbw%=\n\ts_waitcnt vmcnt(%1)\n.Lbw%=:\n\ts_barrier" ::"s"(w), "n"(n) : "memory", "scc")

  f32x16 aq = {}, ak = {}, av = {};
  {
    u32 mk[2][4];
    {
      const u32 e = l31 & 7, on = ((l31 >> 3) & 1) == hf ? ((e & 1) ? 0xffff0000u : 0xffffu) : 0u;
#pragma unroll
      for (int c = 0; c < 2; c++)
#pragma unroll
        for (int i = 0; i < 4; i++) mk[c][i] = ((l31 >> 4) == (u32)c && (e >> 1) == (u32)i) ? on : 0u;
    }
    BASES(wl, (4 * hf + tq) * 192 + tblk * 32 + tp_ * 8);
    u32 tb = L_TAB + l31 * 16;
    asm volatile("" : "+v"(tb));
    u32x2 wr[6][2];
    u32x4 tab[2], dg[2], hb[2][2];
#define W_AD(i) ((((i) / 3) < 12 ? L_W1 + ((i) / 3) * 3072 : L_XR + ((((i) / 3) - 12) >> 2) * XRW + ((((i) / 3) - 12) & 3) * 3072) + ((i) % 3) * 64)
#define W_RD(i) do { wr[(i) % 6][0] = TRR(AT(wl, W_AD(i))); wr[(i) % 6][1] = TRR(AT(wl, W_AD(i) + 8 * 192)); } while (0)
#define DIAG(c, t) u32x4{(t)[0] & mk[c][0], (t)[0] & mk[c][1], (t)[0] & mk[c][2], (t)[0] & mk[c][3]}
    tab[0] = LR128(tb); tab[1] = LR128(tb + 512);
    W_RD(0); W_RD(1); W_RD(2);
    {
      f32x16 h0 = mfma(DIAG(0, tab[0]), xf[0], f32x16{});
      h0 = mfma(DIAG(1, tab[0]), xf[1], h0);
      h0 = mfma(tab[0], bx1, h0);
      hb[0][0] = pack8(h0, 0); hb[0][1] = pack8(h0, 8);
    }
    dg[0] = DIAG(0, tab[1]);
    asm volatile("" : "+v"(dg[0]));
    GAP();
    f32x16 hh;
    sfor<14>(LAM(be) {
      constexpr int be = be_.value, i0 = 6 * be, cur = be & 1, nx = cur ^ 1;
      constexpr bool more = be < 13;
      if constexpr (more) {
        asm volatile("v_mfma_f32_32x32x16_bf16 %0, %1, %2, 0" : "=&v"(hh) : "v"(dg[0]), "v"(xf[2 * be + 2]));
        dg[1] = DIAG(1, tab[nx]);
        asm volatile("" : "+v"(dg[1]));
      }
      GAP();
      aq = mfma(cat(wr[(i0 + 0) % 6][0], wr[(i0 + 0) % 6][1]), hb[cur][0], aq);
      W_RD(i0 + 3);
      if constexpr (be < 12) tab[cur] = LR128(tb + (be + 2) * 512);
      GAP();
      if constexpr (more) asm volatile("v_mfma_f32_32x32x16_bf16 %0, %1, %2, %0" : "+v"(hh) : "v"(dg[1]), "v"(xf[2 * be + 3]));
      W_RD(i0 + 4);
      if constexpr (be < 8) B_REQ(2 * be);
      GAP();
      ak = mfma(cat(wr[(i0 + 1) % 6][0], wr[(i0 + 1) % 6][1]), hb[cur][0], ak);
      W_RD(i0 + 5);
      GAP();
      if constexpr (more) asm volatile("v_mfma_f32_32x32x16_bf16 %0, %1, %2, %0" : "+v"(hh) : "v"(tab[nx]), "v"(bx1));
      if constexpr (be == 1 || be == 3 || be == 7) B_WAIT(7);
      if constexpr (be == 5) { STAMP(24); B_WAIT(7); STAMP(25); }
      if constexpr (be == 9) { STAMP(26); B_WAIT(4); STAMP(27); }
      if constexpr (be == 11) B_WAIT(0);
      if constexpr (more) W_RD(i0 + 6);
      GAP();
      av = mfma(cat(wr[(i0 + 2) % 6][0], wr[(i0 + 2) % 6][1]), hb[cur][0], av);
      if constexpr (more) W_RD(i0 + 7);
      if constexpr (be >= 8 && be < 12) {
        if (njob == 2 && w == 3) glds4<0>(LB + L_XR + XRW + 12288 + (be - 8) * 4096, pv1, mksrd(wd, 1792 * 896), ((bid - 144) * 16 + (be - 8) * 4) * 896, 1024, 896);
      }
      if constexpr (be < 12) { dg[0][0] = tab[cur][0] & mk[0][0]; dg[0][1] = tab[cur][0] & mk[0][1]; asm volatile("" : "+v"(dg[0][0]), "+v"(dg[0][1])); }
      GAP();
      aq = mfma(cat(wr[(i0 + 3) % 6][0], wr[(i0 + 3) % 6][1]), hb[cur][1], aq);
      if constexpr (more) W_RD(i0 + 8);
      if constexpr (be < 12) { dg[0][2] = tab[cur][0] & mk[0][2]; dg[0][3] = tab[cur][0] & mk[0][3]; asm volatile("" : "+v"(dg[0][2]), "+v"(dg[0][3])); }
      GAP();
      ak = mfma(cat(wr[(i0 + 4) % 6][0], wr[(i0 + 4) % 6][1]), hb[cur][1], ak);
      if constexpr (more) {
        asm volatile("" : "+v"(hh));
        hb[nx][0] = pack8(hh, 0);
        asm volatile("" : "+v"(hb[nx][0]));
      }
      if constexpr (be < 8) B_REQ(2 * be + 1);
      GAP();
      av = mfma(cat(wr[(i0 + 5) % 6][0], wr[(i0 + 5) % 6][1]), hb[cur][1], av);
      if constexpr (more) { hb[nx][1] = pack8(hh, 8); asm volatile("" : "+v"(hb[nx][1])); }
      GAP();
    });
  }
  RELANE();
  const u32 odd = j & 1;
  u32x4 aph[2];
  {
#pragma unroll
    for (int s = 0; s < 2; s++) {
      u32x2 t[2];
#pragma unroll
      for (int u = 0; u < 2; u++) {
        int d = 16 * s + 8 * u + 4 * (int)hf + (int)tq - 4 * (int)odd;
        d = d < 0 ? 0 : (d > 27 ? 27 : d);
        t[u] = TRR(L_PHI + d * 56 + tblk * 32 + tp_ * 8);
      }
      aph[s] = cat(t[0], t[1]);
    }
    if (odd) { if (!hf) { aph[0][0] = 0; aph[0][1] = 0; } }
    else { if (hf) { aph[1][2] = 0; aph[1][3] = 0; } }
  }
  BAR();
  STAMP(7); STAMPC(41);
  RELANE();
  const bool dwg = jj < 7;
  const u32 tph = xh, ng = jj;
  {
    f32x16 pk = {};
    pk = mfma(aph[0], pack8s(ak, 0, rs), pk);
    pk = mfma(aph[1], pack8s(ak, 8, rs), pk);
    u32x4 v0 = pack8s(av, 0, rs), v1 = pack8s(av, 8, rs);
    if (odd) { if (!hf) { v0[0] = 0x3F80u; v0[1] = 0; } }
    else { if (hf) { v1[2] = 0x3F80u; v1[3] = 0; } }
    const u32 t = w * 32 + l31;
    const u32 va = (L_VIM + t * 64) | ((hf ^ ((l31 >> 1) & 7)) << 3);
    *(AS3 u32x2*)(L + va) = u32x2{v0[0], v0[1]};
    *(AS3 u32x2*)(L + (va ^ 16)) = u32x2{v0[2], v0[3]};
    *(AS3 u32x2*)(L + (va ^ 32)) = u32x2{v1[0], v1[1]};
    *(AS3 u32x2*)(L + (va ^ 48)) = u32x2{v1[2], v1[3]};
    u32x4 fk[4];
    hedgehog(pk, hf, fk);
    const u32 swp = (l31 & 7) | ((((l31 >> 3) ^ (l31 >> 1)) & 1) << 3);
    const u32 pa = (L_PHK + t * 128) | ((hf ^ swp) << 3);
#pragma unroll
    for (int s = 0; s < 4; s++) {
      const u32 u0 = (s >> 1) * 8 + (s & 1) * 4;
      *(AS3 u32x2*)(L + (pa ^ (u0 << 3))) = u32x2{fk[s][0], fk[s][1]};
      *(AS3 u32x2*)(L + (pa ^ ((u0 + 2) << 3))) = u32x2{fk[s][2], fk[s][3]};
    }
  }
  STAMP(28);
  WAITV(0);
  BAR();
  STAMP(8);
  RELANE();
  if (w < 2) {
    f32x16 kv = {};
    const u32 swa = tq | ((hf ^ (tq >> 1)) & 1) << 3;
    const u32 ua = w * 8 + tblk * 4 + tp_;
    const u32 a0 = L_PHK + (8 * hf + tq) * 128 + ((ua ^ swa) << 3), a1 = L_PHK + (8 * hf + 4 + tq) * 128 + ((ua ^ swa ^ 4) << 3);
    const u32 swb = 4 * hf + (tq >> 1), ub = tblk * 4 + tp_;
    const u32 b0 = L_VIM + (8 * hf + tq) * 64 + ((ub ^ swb) << 3), b1 = L_VIM + (8 * hf + 4 + tq) * 64 + ((ub ^ swb ^ 2) << 3);
    u32x2 xa[8][4];
#pragma unroll
    for (int kk = 0; kk < 8; kk++) { xa[kk][0] = TRR(a0 + kk * 2048); xa[kk][1] = TRR(a1 + kk * 2048); xa[kk][2] = TRR(b0 + kk * 1024); xa[kk][3] = TRR(b1 + kk * 1024); }
    GAP();
#pragma unroll
    for (int kk = 0; kk < 8; kk++) kv = mfma(cat(xa[kk][0], xa[kk][1]), cat(xa[kk][2], xa[kk][3]), kv);
    const u32 base = WS_KVP + ((j * 4 + w * 2) * 16 + mt) * 1024 + lane * 16;
    __builtin_amdgcn_raw_buffer_store_b128(pack8(kv, 0), r_ws, base, 0, 16);
    __builtin_amdgcn_raw_buffer_store_b128(pack8(kv, 8), r_ws, base + 16384, 0, 16);
    WAITV(0);
    if (lane == 0) set_flag(ws, WS_KVF + (j * 2 + w) * 128 + mt * 4, tag);
  } else if (dwg) {
    const u32 which = (w - 2) * 2 + hf;
    if (which < 3) {
      const u32 d = get_granule(ws, WS_MODG + ((which == 0 ? 448 : which == 1 ? 896 : 1120) + ng * 32 + l31) * 8, tag);
      float lo = blo(d), hi = bhi(d);
      if (which == 1) { const u32 sp = cvtpk(1.f + lo, 1.f + hi); lo = blo(sp); hi = bhi(sp); }
      *(AS3 f32x2*)(L + (which == 0 ? L_GA : which == 1 ? L_S2 : L_GB) + l31 * 8) = f32x2{lo, hi};
    }
  }
  STAMP(9);
  RELANE();
  if (w >= 2) {
#pragma unroll 1
    for (u32 jb = 0; jb < njob; jb++) {
      const u32 id = bid + jb * 80, src = L_XR + jb * XRW + 12288;
      const u32 rowl = 4 * hf + tq;
      u32 ad, st, go, gd;
      if (id < 224) {
        ad = src + tblk * 8192 + (rowl >> 1) * 1024 + (rowl & 1) * 448 + (w - 2) * 224 + tp_ * 8; st = 4096;
        go = WS_WGU + ((id & 7) * 2 + (w - 2)) * 196 * 1024; gd = id >> 3;
      } else {
        ad = src + rowl * 1024 + (w - 2) * 448 + tblk * 32 + tp_ * 8; st = 8192;
        go = WS_WD + ((w - 2) * 7 * 112 + (id - 224)) * 1024; gd = 0;
      }
      u32x2 t[7][2];
#pragma unroll
      for (int i = 0; i < 7; i++) { const u32 o = id < 224 ? i * 32 : i * 64; t[i][0] = TRR(ad + o); t[i][1] = TRR(ad + o + st); }
#pragma unroll
      for (int i = 0; i < 7; i++) {
        const u32 g = id < 224 ? (i < 4 ? gd * 4 + i : 112 + gd * 3 + i - 4) : i * 112;
        __builtin_amdgcn_raw_buffer_store_b128(cat(t[i][0], t[i][1]), r_ws, lane * 16, go + g * 1024, 16);
      }
    }
  }
  if (dwg && w >= 2) {
    const u32x4 s_wo = mksrd(wo, 448 * 896), s_x = mksrd(x, 2048 * 896);
    const u32 r = lane >> 3, vo = r * 896 + (((lane & 7) ^ (((r >> 1) & 1) << 2)) << 4);
    const u32 p0 = (w - 2) * 28;
#pragma unroll
    for (int i = 0; i < 4; i++) glds7<0>(LB + L_WO + (p0 + i * 7) * 1024, vo, s_wo, (p0 + i * 7) * 7168 + ng * 128, 1024, 7168);
    const u32 vx = r * 896 + (((lane & 7) ^ r) << 4);
    glds4<0>(LB + L_XRES + (w - 2) * 4096, vx, s_x, (mt * 128 + tph * 64 + (w - 2) * 32) * 896 + ng * 128, 1024, 7168);
  }
  RELANE();
  u32x4 fq[4];
  {
    f32x16 pq = {};
    pq = mfma(aph[0], pack8s(aq, 0, rs), pq);
    pq = mfma(aph[1], pack8s(aq, 8, rs), pq);
    hedgehog(pq, hf, fq);
  }
  STAMP(10);

  {
    poll_flags(ws, WS_KVF + (j * 2 + (w >> 1)) * 128 + (lane & 15) * 4, lane < 16, tag);
    RAWBAR();
    if (tid == 0) {
      set_flag(ws, bid < 224 ? WS_PF + (bid & 7) * 128 + (bid >> 3) * 4 : WS_PD + (bid - 224) * 4, tag);
      if (bid >= 176) set_flag(ws, WS_PD + (bid - 144) * 4, tag);
    }
    STAMP(11);
    u32x4 pt[16];
#pragma unroll
    for (int i = 0; i < 16; i++) pt[i] = __builtin_amdgcn_raw_buffer_load_b128(r_ws, lane * 16, WS_KVP + ((j * 4 + w) * 16 + ((i + mt) & 15)) * 1024, 16);
    STAMP(29);
    float sm[8] = {};
    u32 one_lo = 0x00003F80u, one_hi = 0x3F800000u;
    asm("" : "+s"(one_lo), "+s"(one_hi));
#pragma unroll
    for (int i = 0; i < 16; i++)
#pragma unroll
      for (int e = 0; e < 4; e++) { sm[2 * e] = dot2(pt[i][e], one_lo, sm[2 * e]); sm[2 * e + 1] = dot2(pt[i][e], one_hi, sm[2 * e + 1]); }
    *(AS3 u32x4*)(L + L_KVS + w * 1024 + lane * 16) = u32x4{cvtpk(sm[0], sm[1]), cvtpk(sm[2], sm[3]), cvtpk(sm[4], sm[5]), cvtpk(sm[6], sm[7])};
    STAMP(30);
    BAR();
    STAMP(31);
    f32x16 o = {};
#pragma unroll
    for (int s = 0; s < 4; s++) o = mfma(LR128(L_KVS + s * 1024 + lane * 16), fq[s], o);
    auto dd = __builtin_amdgcn_permlane32_swap(asu(odd ? o[0] : o[12]), asu(odd ? o[0] : o[12]), false, false);
    const float inv = __builtin_amdgcn_rcpf(asf(odd ? dd[0] : dd[1]) + 1e-6f);
    u32x4 o0 = pack8s(o, 0, inv), o1 = pack8s(o, 8, inv);
    if (odd) { if (!hf) { o0[0] = 0; o0[1] = 0; } }
    else { if (hf) { o1[2] = 0; o1[3] = 0; } }
    const u32 base = WS_OFR + (((mt * 4 + w) * 16 + j) * 2) * 1024 + lane * 16;
    __builtin_amdgcn_raw_buffer_store_b128(o0, r_ws, base, 0, 16);
    __builtin_amdgcn_raw_buffer_store_b128(o1, r_ws, base + 1024, 0, 16);
    STAMP(32);
    const u32 pfo = WS_PF + (j >> 1) * 128 + (l31 < 28 ? l31 : 0) * 4;
    u32 pf;
    asm volatile("buffer_load_dword %0, %1, %2, 0 offen sc1\n\ts_waitcnt vmcnt(0)" : "=v"(pf) : "v"(pfo), "s"(s_ws) : "memory");
    if (lane == 0) set_flag(ws, WS_OF + (mt * 4 + w) * 128 + j * 4, tag);
    STAMP(12);
    if (!__all(pf == tag)) poll_flags(ws, pfo, true, tag);
  }
  STAMP(13);
  RELANE();
  const u32 ring_src = WS_WGU + j * 196 * 1024;
#define RING_LOAD(p0, n) sfor<n>(LAM(i) { constexpr int i = i_.value; glds1(LB + (((p0) + 4 * i) % RING) * 1024 + w * 1024, lane * 16, s_ws, ring_src + ((p0) + 4 * i) * 1024 + w * 1024); })

  const u32 nh = w & 1, th = w >> 1;
  u32x4 x1p[2];
  x1p[0] = u32x4{0, 0, 0, 0}; x1p[1] = x1p[0];
  if (dwg) {
    RING_LOAD(0, PRE_A / 4);
    const u32 tile = mt * 4 + tph * 2 + th;
    poll_flags(ws, WS_OF + tile * 128 + (nh * 8 + (lane & 7)) * 4, lane < 8, tag);
    STAMP(14);
    u32x4 of[16];
#pragma unroll
    for (int i = 0; i < 16; i++) of[i] = __builtin_amdgcn_raw_buffer_load_b128(r_ws, lane * 16, WS_OFR + (tile * 32 + nh * 16 + i) * 1024, 16);
    f32x16 acc[2] = {};
    {
      u32 wl[2];
#pragma unroll
      for (int t = 0; t < 2; t++) wl[t] = L_WO + nh * 224 * 128 + (4 * hf + tq) * 128 + (((t * 4 + tblk * 2 + (tp_ >> 1)) ^ ((tq >> 1) << 2)) << 4) + (tp_ & 1) * 8;
      asm volatile("" : "+v"(wl[0]), "+v"(wl[1]));
      u32x2 wr[6][2];
#define O_ROW(n) (28 * ((n) >> 2) - 4 * (((n) >> 2) & 1) + 16 * (((n) >> 1) & 1))
#define O_RD(n) do { wr[(n) % 6][0] = TRR(wl[(n) & 1] + O_ROW(n) * 128); wr[(n) % 6][1] = TRR(wl[(n) & 1] + (O_ROW(n) + 8) * 128); } while (0)
      O_RD(0); O_RD(1); O_RD(2); O_RD(3);
      GAP();
      sfor<32>(LAM(n) {
        constexpr int n = n_.value;
        acc[n & 1] = mfma(cat(wr[n % 6][0], wr[n % 6][1]), of[n >> 1], acc[n & 1]);
        if constexpr (n + 4 < 32) O_RD(n + 4);
        GAP();
      });
    }
    STAMP(15);
    BAR();
    {
      const f32x16& snd = nh ? acc[0] : acc[1];
      const u32 ad = L_DEX + (w ^ 1) * 4096 + lane * 16;
#pragma unroll
      for (int g = 0; g < 4; g++) *(AS3 f32x4*)(L + ad + g * 1024) = f32x4{snd[4 * g], snd[4 * g + 1], snd[4 * g + 2], snd[4 * g + 3]};
    }
    BAR();
    u32x4 yp[2];
    float su = 0.f, sq = 0.f;
#pragma unroll
    for (int g = 0; g < 4; g++) {
      const f32x4 ot = *(AS3 f32x4*)(L + L_DEX + w * 4096 + lane * 16 + g * 1024);
      const f32x4 ga = *(AS3 f32x4*)(L + L_GA + (nh * 32 + g * 8 + 4 * hf) * 4), s2 = *(AS3 f32x4*)(L + L_S2 + (nh * 32 + g * 8 + 4 * hf) * 4);
      const u32x2 xr = LR64(L_XRES + (th * 32 + l31) * 128 + (((nh * 4 + g) ^ (l31 & 7)) << 4) + hf * 8);
      float a[4];
#pragma unroll
      for (int e = 0; e < 4; e++) a[e] = (nh ? acc[1][4 * g + e] : acc[0][4 * g + e]) + ot[e];
      const u32 p0 = cvtpk(__builtin_fmaf(ga[0], a[0], blo(xr[0])), __builtin_fmaf(ga[1], a[1], bhi(xr[0])));
      const u32 p1 = cvtpk(__builtin_fmaf(ga[2], a[2], blo(xr[1])), __builtin_fmaf(ga[3], a[3], bhi(xr[1])));
      x1p[g >> 1][(g & 1) * 2] = p0; x1p[g >> 1][(g & 1) * 2 + 1] = p1;
      su = dot2(p0, 0x3F803F80u, su); su = dot2(p1, 0x3F803F80u, su);
      sq = dot2(p0, p0, sq); sq = dot2(p1, p1, sq);
      yp[g >> 1][(g & 1) * 2] = cvtpk(blo(p0) * s2[0], bhi(p0) * s2[1]);
      yp[g >> 1][(g & 1) * 2 + 1] = cvtpk(blo(p1) * s2[2], bhi(p1) * s2[3]);
    }
    su = swsum(su); sq = swsum(sq);
    const u32 nt = ng * 2 + nh;
    __builtin_amdgcn_raw_buffer_store_b128(yp[0], r_ws, WS_YF + (tile * 28 + nt * 2) * 1024 + lane * 16, 0, 16);
    __builtin_amdgcn_raw_buffer_store_b128(yp[1], r_ws, WS_YF + (tile * 28 + nt * 2 + 1) * 1024 + lane * 16, 0, 16);
    if (hf == 0) __builtin_amdgcn_raw_buffer_store_b64(u32x2{asu(su), asu(sq)}, r_ws, WS_STAT + (tile * 14 + nt) * 256 + l31 * 8, 0, 16);
    STAMP(33);
    WAITV(0);
    STAMP(34);
    if (lane == 0) set_flag(ws, WS_XF + tile * 128 + nt * 4, tag);
    BAR();
    STAMP(16);
    RING_LOAD(PRE_A, (148 - PRE_A) / 4);
    STAMP(35);
  } else {
    const u32 mat = (mt >> 1) & 1, ft = xh * 8 + (mt >> 2) * 2 + (mt & 1);
    const auto r_m = RSRC(mat ? wu : wg, 448 * 3584);
    const u32 ch = lane % 14, sub = lane / 14;
    u32x4 wv[28];
#pragma unroll
    for (int i = 0; i < 28; i++) wv[i] = __builtin_amdgcn_raw_buffer_load_b128(r_m, lane < 56 ? (i * 16 + w * 4 + sub) * 3584 + ft * 224 + ch * 16 : OOB, 0, 0);
    if (tid < 224) {
      const u32 cb = get_granule(ws, WS_MODG + (896 + tid) * 8, tag), sb = get_granule(ws, WS_MODG + (672 + tid) * 8, tag), sp = cvtpk(1.f + blo(cb), 1.f + bhi(cb));
      *(AS3 u32x4*)(L + L_P1T + tid * 32) = u32x4{sp & 0xffffu, sp << 16, sb & 0xffffu, sb << 16};
      *(AS3 u32x4*)(L + L_P1T + tid * 32 + 16) = u32x4{sp >> 16, sp & 0xffff0000u, sb >> 16, sb & 0xffff0000u};
    }
    BAR();
    float au[8] = {}, ab[8] = {};
    const u32 subc = sub < 4 ? sub : 0;
#pragma unroll
    for (int i = 0; i < 28; i++) {
      const u32x4 t = LR128(L_P1T + (i * 16 + w * 4 + subc) * 16);
#pragma unroll
      for (int e = 0; e < 4; e++) {
        au[2 * e] = dot2(wv[i][e], t[0], au[2 * e]); au[2 * e + 1] = dot2(wv[i][e], t[1], au[2 * e + 1]);
        ab[2 * e] = dot2(wv[i][e], t[2], ab[2 * e]); ab[2 * e + 1] = dot2(wv[i][e], t[3], ab[2 * e + 1]);
      }
    }
    if (lane < 56) {
      const u32 ro = L_P1R + (w * 4 + sub) * 896 + ch * 32;
      *(AS3 f32x4*)(L + ro) = f32x4{au[0], au[1], au[2], au[3]};
      *(AS3 f32x4*)(L + ro + 16) = f32x4{au[4], au[5], au[6], au[7]};
      *(AS3 f32x4*)(L + ro + 448) = f32x4{ab[0], ab[1], ab[2], ab[3]};
      *(AS3 f32x4*)(L + ro + 464) = f32x4{ab[4], ab[5], ab[6], ab[7]};
    }
    BAR();
    if (tid < 112) {
      float u = 0.f, b = 0.f;
#pragma unroll
      for (int i = 0; i < 16; i++) { u += asf(LR32(L_P1R + i * 896 + tid * 4)); b += asf(LR32(L_P1R + i * 896 + 448 + tid * 4)); }
      const u32 h2 = cvtpk(u, b);
      const u32 l2 = cvtpk(u - blo(h2), b - bhi(h2));
      const u32 uh = h2 & 0xffffu, bh = h2 >> 16, ul = l2 & 0xffffu, bl = l2 >> 16;
      __builtin_amdgcn_raw_buffer_store_b128(u32x4{uh * 0x10001u, bh * 0x10001u, ul, bl}, r_ws,
                                             WS_EXF + ((ft * 7 + (tid >> 4)) * 32 + (tid & 15) + 16 * mat) * 16, 0, 16);
    }
    WAITV(0);
    BAR();
    if (tid == 0) set_flag(ws, WS_UF + (ft * 2 + mat) * 4, tag);
    RING_LOAD(0, 37);
  }

  RELANE();
  u32x4 wdf[56];
  {
    const u32 tile = mt * 4 + w;
    if (w < 2) glds1(LB + (148 + w) * 1024, lane * 16, s_ws, ring_src + (148 + w) * 1024);
    u32x4 ex[7];
    poll_flags(ws, WS_XF + tile * 128 + (lane & 15) * 4, lane < 14, tag);
    if (bid == 0 && tid == 0) set_flag(ws, WS_EPOCH, tag);
    STAMP(17);
    u32x4 yf[28];
#pragma unroll
    for (int s = 0; s < 28; s++) yf[s] = __builtin_amdgcn_raw_buffer_load_b128(r_ws, lane * 16, WS_YF + (tile * 28 + s) * 1024, 16);
    u32x2 st[7];
#pragma unroll
    for (int i = 0; i < 7; i++) st[i] = __builtin_amdgcn_raw_buffer_load_b64(r_ws, l31 * 8 + hf * 7 * 256, WS_STAT + (tile * 14 + i) * 256, 16);
    VWAITN(34);
    RAWBAR();
    STAMP(18); STAMPC(42);
    f32x16 a1[4], a2[3];
    u32x4 bx, hv[4];
    u32 fl0, fl1;
    const u32 flo0 = lane < 2 ? WS_UF + (j * 2 + lane) * 4 : WS_PD + (lane - 2) * 4, flo1 = WS_PD + (lane < 50 ? 62 + lane : 62) * 4;
    constexpr int PD = 6, AR = 8;
    u32x4 ar[AR];
    BASES(lb, lane * 16);
#define E_RD(n) LR128(AT(lb, ((n) % RING) * 1024))
    const u32 wd_src = WS_WD + (((dwg ? ng : 0) * 2 + nh) * 112 + th * 56) * 1024, wd_vo = dwg ? lane * 16 : OOB;
#define WD_KS(q) ((q) < 32 ? ((q) >> 2) * 7 + ((q) & 3) : (((q) - 32) / 3) * 7 + 4 + ((q) - 32) % 3)
    sfor<PD>(LAM(n) { constexpr int n = n_.value; ar[n] = E_RD(n); });
    GAP();
    sfor<196>(LAM(n) {
      constexpr int n = n_.value;
      if constexpr (n < 112) a1[n & 3] = mfma(ar[n % AR], yf[n >> 2], n < 4 ? f32x16{} : a1[n & 3]);
      else a2[(n - 112) % 3] = mfma(ar[n % AR], yf[(n - 112) / 3], n < 115 ? f32x16{} : a2[(n - 112) % 3]);
      if constexpr (n == 72) { STAMP(36); }
      if constexpr (n == 73) { STAMP(37); }
      if constexpr (n == 144) { STAMP(38); }
      if constexpr (n == 145) { STAMP(39); }
      if constexpr (n == 72) {
        VWAIT0();
        RAWBAR();
        float su = 0.f, sq = 0.f;
#pragma unroll
        for (int i = 0; i < 7; i++) { su += asf(st[i][0]); sq += asf(st[i][1]); }
        const float m = swsum(su) * (1.f / 448.f), var = fmaxf(swsum(sq) * (1.f / 448.f) - m * m, 0.f) + 1e-5f;
        const float rr = __builtin_amdgcn_rsqf(var);
        if (hf == 0) *(AS3 float*)(L + L_R2 + (w * 32 + l31) * 4) = rr * rr;
        const u32 h2 = cvtpk(-m, var * rr), l2 = cvtpk(-m - blo(h2), var * rr - bhi(h2));
        bx = hf ? u32x4{0, 0, 0, 0} : u32x4{(h2 & 0xffffu) | (l2 << 16), (h2 >> 16) | (l2 & 0xffff0000u), h2 & 0xffffu, h2 >> 16};
      }
      if constexpr (n == 72) asm volatile("buffer_load_dword %0, %2, %4, 0 offen sc1\n\tbuffer_load_dword %1, %3, %4, 0 offen sc1" : "=&v"(fl0), "=&v"(fl1) : "v"(flo0), "v"(flo1), "s"(s_ws) : "memory");
      if constexpr (n == 85) {
        asm volatile("s_waitcnt vmcnt(12)" : "+v"(fl0), "+v"(fl1)::"memory");
        if (!__all(fl0 == tag && fl1 == tag)) { poll_flags(ws, flo0, true, tag); poll_flags(ws, flo1, true, tag); }
      }
      if constexpr (n >= 86 && n < 93) ex[n - 86] = __builtin_amdgcn_raw_buffer_load_b128(r_ws, hf ? OOB : l31 * 16, WS_EXF + (j * 7 + n - 86) * 512, 16);
      if constexpr (n >= 73 && n < 85) {
        constexpr int i = n - 73;
        if constexpr (i < 11) glds1(LB + (4 * i) * 1024 + w * 1024, lane * 16, s_ws, ring_src + (150 + 4 * i) * 1024 + w * 1024);
        else glds1(LB + (4 * i) * 1024 + w * 1024, w < 2 ? lane * 16 : OOB, s_ws, ring_src + (150 + 4 * i) * 1024 + w * 1024);
      }
      if constexpr (n >= 85 && (n - 85) % 2 == 0) {
        constexpr int q = (n - 85) / 2;
        wdf[q] = __builtin_amdgcn_raw_buffer_load_b128(r_ws, wd_vo, wd_src + WD_KS(q) * 1024, 16);
      }
      if constexpr (n + PD == 150) { WAITV(37); RAWBAR(); }
      if constexpr (n + PD < 196) ar[(n + PD) % AR] = E_RD(n + PD);
      if constexpr (n >= 112 && n < 116) a1[n - 112] = mfma(ex[n - 112], bx, a1[n - 112]);
      if constexpr (n >= 120 && n < 136) {
        constexpr int t = (n - 120) >> 2, d = (n - 120) & 3;
        hv[t][d] = cvtpk(a1[t][2 * d] * a1[t][2 * d + 8], a1[t][2 * d + 1] * a1[t][2 * d + 9]);
        if constexpr (d == 3) __builtin_amdgcn_raw_buffer_store_b128(hv[t], r_ws, WS_HH + (tile * 112 + j * 7 + t) * 1024 + lane * 16, 0, 16);
      }
      if constexpr (n == 160) { VWAITN(12); if (lane == 0) set_flag(ws, WS_HF + tile * 128 + j * 4, tag); }
      GAP();
    });
    STAMP(19); STAMPC(43);
    sfor<3>(LAM(t) { constexpr int t = t_.value; a2[t] = mfma(ex[4 + t], bx, a2[t]); });
    sfor<3>(LAM(t) {
      constexpr int t = t_.value;
      const u32x4 h = u32x4{cvtpk(a2[t][0] * a2[t][8], a2[t][1] * a2[t][9]), cvtpk(a2[t][2] * a2[t][10], a2[t][3] * a2[t][11]),
                            cvtpk(a2[t][4] * a2[t][12], a2[t][5] * a2[t][13]), cvtpk(a2[t][6] * a2[t][14], a2[t][7] * a2[t][15])};
      __builtin_amdgcn_raw_buffer_store_b128(h, r_ws, WS_HH + (tile * 112 + j * 7 + 4 + t) * 1024 + lane * 16, 0, 16);
    });
    VWAIT0();
    if (lane == 0) set_flag(ws, WS_HF + tile * 128 + 64 + j * 4, tag);
  }
  STAMP(20);

  RELANE();
  if (dwg) {
    const u32 kh = th;
    BAR();
    const u32 ltile = mt * 4 + tph * 2 + nh;
    poll_flags(ws, WS_HF + ltile * 128 + (kh * 8 + (lane & 7)) * 4, lane < 8, tag);
    STAMP(21); STAMPC(44);
    const u32 ldst = LB + (kh * 2 + nh) * 8192, lsrc = WS_HH + (ltile * 112 + kh * 56) * 1024;
#define H_LD(q) glds1(ldst + (((q) >> 3) & 3) * 32768 + ((q) & 7) * 1024, lane * 16, s_ws, lsrc + WD_KS(q) * 1024)
    sfor<16>(LAM(q) { constexpr int q = q_.value; H_LD(q); });
    f32x16 af[2];
    constexpr int PD = 6, AR = 8;
    u32x4 br[AR];
    BASES(lb, lane * 16 + kh * 16384);
#define F_RD(n) LR128(AT(lb, (((n) >> 4) & 3) * 32768 + ((n) & 1) * 8192 + (((n) >> 1) & 7) * 1024))
    WAITV(8);
    RAWBAR();
    sfor<PD>(LAM(n) { constexpr int n = n_.value; br[n] = F_RD(n); });
    GAP();
    u32 hf2;
    sfor<112>(LAM(n) {
      constexpr int n = n_.value;
      af[n & 1] = mfma(wdf[n >> 1], br[n % AR], n < 2 ? f32x16{} : af[n & 1]);
      if constexpr (n < 32 && (n & 1) == 0) H_LD(16 + (n >> 1));
      if constexpr (n == 33) asm volatile("buffer_load_dword %0, %1, %2, 0 offen sc1" : "=v"(hf2) : "v"(WS_HF + ltile * 128 + 64 + (kh * 8 + (lane & 7)) * 4), "s"(s_ws) : "memory");
      if constexpr (n + PD == 16) { WAITV(6); RAWBAR(); }
      if constexpr (n + PD == 32) { WAITV(6); RAWBAR(); }
      if constexpr (n + PD == 48) {
        asm volatile("s_waitcnt vmcnt(0)" : "+v"(hf2)::"memory");
        if (!__all(hf2 == tag)) poll_flags(ws, WS_HF + ltile * 128 + 64 + (kh * 8 + (lane & 7)) * 4, lane < 8, tag);
        RAWBAR();
      }
      if constexpr (n >= 43 && n < 59) H_LD(32 + (n - 43));
      if constexpr (n + PD == 64) { WAITV(8); RAWBAR(); }
      if constexpr (n >= 59 && n < 67) H_LD(48 + (n - 59));
      if constexpr (n + PD == 80) { WAITV(8); RAWBAR(); }
      if constexpr (n + PD == 96) { WAITV(0); RAWBAR(); }
      if constexpr (n + PD < 112) br[(n + PD) % AR] = F_RD(n + PD);
      GAP();
    });
    STAMP(22); STAMPC(45);
    {
      const f32x16& snd = kh ? af[0] : af[1];
      const u32 ad = L_FEX + (w ^ 2) * 4096 + lane * 16;
#pragma unroll
      for (int g = 0; g < 4; g++) *(AS3 f32x4*)(L + ad + g * 1024) = f32x4{snd[4 * g], snd[4 * g + 1], snd[4 * g + 2], snd[4 * g + 3]};
    }
    BAR();
    const float rr2 = *(AS3 float*)(L + L_R2 + (tph * 64 + th * 32 + l31) * 4);
    u32 po[8];
#pragma unroll
    for (int g = 0; g < 4; g++) {
      const f32x4 ot = *(AS3 f32x4*)(L + L_FEX + w * 4096 + lane * 16 + g * 1024);
      const f32x4 gb = *(AS3 f32x4*)(L + L_GB + (nh * 32 + g * 8 + 4 * hf) * 4);
      float v[4];
#pragma unroll
      for (int e = 0; e < 4; e++) v[e] = ((kh ? af[1][4 * g + e] : af[0][4 * g + e]) + ot[e]) * rr2 * gb[e];
      const u32 p0 = x1p[g >> 1][(g & 1) * 2], p1 = x1p[g >> 1][(g & 1) * 2 + 1];
      po[2 * g] = cvtpk(blo(p0) + v[0], bhi(p0) + v[1]);
      po[2 * g + 1] = cvtpk(blo(p1) + v[2], bhi(p1) + v[3]);
    }
    const auto r_o = RSRC(out, 2048 * 896);
    const u32 oo = (mt * 128 + tph * 64 + th * 32 + l31) * 896 + (ng * 64 + nh * 32) * 2 + hf * 16;
#pragma unroll
    for (int g2 = 0; g2 < 2; g2++) {
      auto s0 = __builtin_amdgcn_permlane32_swap(po[4 * g2], po[4 * g2 + 2], false, false);
      auto s1 = __builtin_amdgcn_permlane32_swap(po[4 * g2 + 1], po[4 * g2 + 3], false, false);
      __builtin_amdgcn_raw_buffer_store_b128(u32x4{s0[0], s1[0], s0[1], s1[1]}, r_o, oo + g2 * 32, 0, 0);
    }
  }
  STAMP(23);
  STAMP_FLUSH();
}

extern "C" void kernel_launch(void* const* d_in, const int* in_sizes, int n_in, void* d_out, int out_size, void* d_ws, size_t ws_size, hipStream_t stream) {
  (void)in_sizes; (void)n_in; (void)out_size; (void)ws_size;
  (void)hipFuncSetAttribute((const void*)fused_block, hipFuncAttributeMaxDynamicSharedMemorySize, LDS_BYTES);
  fused_block<<<256, 256, LDS_BYTES, stream>>>((const u16*)d_in[0], (const u16*)d_in[1], (const u16*)d_in[2], (const u16*)d_in[3], (const u16*)d_in[5], (const u16*)d_in[6],
                                               (const u16*)d_in[7], (const u16*)d_in[8], (const u16*)d_in[9], (u16*)d_out, (char*)d_ws);
}
```

```cpp
#include <hip/hip_runtime.h>
#include <stdint.h>
#include <utility>

typedef unsigned u32;
typedef unsigned short u16;
typedef unsigned long long u64;
typedef u32 u32x4 __attribute__((ext_vector_type(4), may_alias));
typedef u32 u32x2 __attribute__((ext_vector_type(2), may_alias));
typedef float f32x16 __attribute__((ext_vector_type(16)));
typedef float f32x4 __attribute__((ext_vector_type(4), may_alias));
typedef float f32x2 __attribute__((ext_vector_type(2), may_alias));
typedef __bf16 bf16x8 __attribute__((ext_vector_type(8)));
typedef __bf16 bf16x2 __attribute__((ext_vector_type(2)));
typedef short s16x4 __attribute__((ext_vector_type(4)));
#define DI __device__ __forceinline__
#define AS3 __attribute__((address_space(3)))
#define AS1 __attribute__((address_space(1)))
#define RLX_AGENT __ATOMIC_RELAXED, __HIP_MEMORY_SCOPE_AGENT

constexpr u32 WS_EPOCH = 0, WS_TMO = 256;
constexpr u32 WS_MODG = 4096;
constexpr u32 WS_KVF = 16384;
constexpr u32 WS_OF = 20480;
constexpr u32 WS_XF = 28672;
constexpr u32 WS_PF = 36864;
constexpr u32 WS_UF = 38912;
constexpr u32 WS_PD = 39168;
constexpr u32 WS_HF = 49152;
constexpr u32 WS_KVP = 65536;
constexpr u32 WS_OFR = WS_KVP + (1u << 20);
constexpr u32 WS_YF = WS_OFR + (2u << 20);
constexpr u32 WS_STAT = WS_YF + 64 * 28 * 1024;
constexpr u32 WS_HH = WS_STAT + 64 * 14 * 256;
constexpr u32 WS_WGU = WS_HH + 64 * 112 * 1024;
constexpr u32 WS_WD = WS_WGU + 3136 * 1024;
constexpr u32 WS_EXF = WS_WD + 1568 * 1024;
constexpr u32 WS_END = WS_EXF + 112 * 512;
constexpr u32 WS_DBG = 24u << 20;

constexpr u32 L_XR = 0, XRW = 28672;
constexpr u32 L_W1 = 114688;
constexpr u32 L_PHI = 151552;
constexpr u32 L_TAB = 153600;
constexpr u32 L_RED = 160768;
constexpr u32 L_PHK = 57344, L_VIM = 73728, L_KVS = 81920;
constexpr u32 RING = 150;
constexpr u32 L_WO = 94 * 1024;
constexpr u32 L_DEX = 134 * 1024;
constexpr u32 L_XRES = 153600;
constexpr u32 L_GA = 161792, L_S2 = 162048, L_GB = 162304, L_R2 = 162560;
constexpr u32 L_P1T = 0, L_P1R = 8192;
constexpr u32 L_FEX = 32 * 1024;
constexpr u32 LDS_BYTES = 163840;

constexpr u32 SPIN_MAX = 100000;
constexpr u32 CODE_KB = 45;
constexpr int WD_A = 8, WD_B = 16, WD_C = 8, WD_P = WD_A + WD_B + WD_C;
constexpr int WD_F = 0, WD_E = 56 - WD_P - WD_F;
constexpr int BP = 8;
constexpr int GB = 5;
constexpr int PRE_A = 80;

template <class F, int... Is> DI void sfor_(F&& f, std::integer_sequence<int, Is...>) { (f(std::integral_constant<int, Is>{}), ...); }
template <int N, class F> DI void sfor(F&& f) { sfor_(f, std::make_integer_sequence<int, N>{}); }
#define LAM(i) [&](auto i##_) __attribute__((always_inline))

DI f32x16 mfma(u32x4 a, u32x4 b, f32x16 c) {
  return __builtin_amdgcn_mfma_f32_32x32x16_bf16(__builtin_bit_cast(bf16x8, a), __builtin_bit_cast(bf16x8, b), c, 0, 0, 0);
}
DI float asf(u32 x) { return __builtin_bit_cast(float, x); }
DI u32 asu(float x) { return __builtin_bit_cast(u32, x); }
DI u32 cvtpk(float lo, float hi) { f32x2 v = {lo, hi}; return __builtin_bit_cast(u32, __builtin_convertvector(v, bf16x2)); }
DI float dot2(u32 a, u32 b, float c) { return __builtin_amdgcn_fdot2_f32_bf16(__builtin_bit_cast(bf16x2, a), __builtin_bit_cast(bf16x2, b), c, false); }
DI float blo(u32 d) { return asf(d << 16); }
DI float bhi(u32 d) { return asf(d & 0xffff0000u); }
DI u32x4 pack8(const f32x16& c, int o) { return u32x4{cvtpk(c[o], c[o + 1]), cvtpk(c[o + 2], c[o + 3]), cvtpk(c[o + 4], c[o + 5]), cvtpk(c[o + 6], c[o + 7])}; }
DI u32x4 cat(u32x2 a, u32x2 b) { return u32x4{a[0], a[1], b[0], b[1]}; }
DI float swsum(float v) { auto r = __builtin_amdgcn_permlane32_swap(asu(v), asu(v), false, false); return asf(r[0]) + asf(r[1]); }
DI float swmax(float v) { auto r = __builtin_amdgcn_permlane32_swap(asu(v), asu(v), false, false); return fmaxf(asf(r[0]), asf(r[1])); }
DI float swmin(float v) { auto r = __builtin_amdgcn_permlane32_swap(asu(v), asu(v), false, false); return fminf(asf(r[0]), asf(r[1])); }
DI u32x4 mksrd(const void* p, u32 bytes) { u64 a = (u64)p; return u32x4{(u32)a, (u32)(a >> 32) & 0xffffu, bytes, 0x00020000u}; }
#define RSRC(p, bytes) __builtin_amdgcn_make_buffer_rsrc((void*)(p), 0, (bytes), 0x00020000)
#define WAITV(n) asm volatile("s_waitcnt vmcnt(%0)" ::"n"(n) : "memory")
#define BAR() asm volatile("s_waitcnt lgkmcnt(0)\n\ts_barrier" ::: "memory")
#define RAWBAR() asm volatile("s_barrier" ::: "memory")
#define GAP() __builtin_amdgcn_sched_barrier(0)
#define VWAITN(n) __builtin_amdgcn_s_waitcnt(((n) & 15) | (((n) >> 4) << 14) | 0x0F70)
#define VWAIT0() VWAITN(0)
constexpr u32 OOB = 0x7fff0000u;
DI void glds(u32 m0, u32 voff, u32x4 srd, u32 soff) {
  asm volatile("s_mov_b32 m0, %0\n\ts_nop 0\n\tbuffer_load_dwordx4 %1, %2, %3 offen lds" ::"s"(m0), "v"(voff), "s"(srd), "s"(soff) : "memory", "m0");
}
DI void glds1(u32 m0, u32 voff, u32x4 srd, u32 soff) {
  asm volatile("s_mov_b32 m0, %0\n\ts_nop 0\n\tbuffer_load_dwordx4 %1, %2, %3 offen sc1 lds" ::"s"(m0), "v"(voff), "s"(srd), "s"(soff) : "memory", "m0");
}
DI void aload128(u32x4& d, u32 voff, u32x4 srd, u32 soff) {
  asm volatile("buffer_load_dwordx4 %0, %1, %2, %3 offen sc1" : "=&v"(d) : "v"(voff), "s"(srd), "s"(soff) : "memory");
}
DI void aload64(u32x2& d, u32 voff, u32x4 srd) { asm volatile("buffer_load_dwordx2 %0, %1, %2, 0 offen sc1" : "=&v"(d) : "v"(voff), "s"(srd) : "memory"); }
DI void aload32(u32& d, u32 voff, u32x4 srd) { asm volatile("buffer_load_dword %0, %1, %2, 0 offen sc1" : "=&v"(d) : "v"(voff), "s"(srd) : "memory"); }
template <int OFF> DI void aflag(u32 vtag, u32 voff, u32x4 srd) { asm volatile("buffer_store_dword %0, %1, %2, 0 offen offset:%3 sc1" ::"v"(vtag), "v"(voff), "s"(srd), "n"(OFF) : "memory"); }
DI void keep_acc(f32x16& a) { asm volatile("" : "+a"(a)); }
template <int N, class T> DI void await1(T& a) { asm volatile("s_waitcnt vmcnt(%1)" : "+v"(a) : "n"(N) : "memory"); }
template <int N, class T> DI void await4(T& a, T& b, T& c, T& d) { asm volatile("s_waitcnt vmcnt(%4)" : "+v"(a), "+v"(b), "+v"(c), "+v"(d) : "n"(N) : "memory"); }
#define GN_STEP(SC) "s_add_u32 m0, m0, %[dm]\n\ts_add_u32 %[so], %[so], %[dg]\n\tbuffer_load_dwordx4 %[v], %[srd], %[so] offen " SC "lds\n\t"
#define GN_HEAD(SC) "s_mov_b32 m0, %[m]\n\ts_nop 0\n\tbuffer_load_dwordx4 %[v], %[srd], %[so] offen " SC "lds\n\t"
#define GN_ARGS : [so] "+s"(soff) : [m] "s"(m0), [v] "v"(voff), [srd] "s"(srd), [dm] "s"(dm), [dg] "s"(dg) : "memory", "scc", "m0"
template <int SC1> DI void glds7(u32 m0, u32 voff, u32x4 srd, u32 soff, u32 dm, u32 dg) {
  if constexpr (SC1) asm volatile(GN_HEAD("sc1 ") GN_STEP("sc1 ") GN_STEP("sc1 ") GN_STEP("sc1 ") GN_STEP("sc1 ") GN_STEP("sc1 ") GN_STEP("sc1 ") GN_ARGS);
  else asm volatile(GN_HEAD("") GN_STEP("") GN_STEP("") GN_STEP("") GN_STEP("") GN_STEP("") GN_STEP("") GN_ARGS);
}
template <int SC1> DI void glds4(u32 m0, u32 voff, u32x4 srd, u32 soff, u32 dm, u32 dg) {
  if constexpr (SC1) asm volatile(GN_HEAD("sc1 ") GN_STEP("sc1 ") GN_STEP("sc1 ") GN_STEP("sc1 ") GN_ARGS);
  else asm volatile(GN_HEAD("") GN_STEP("") GN_STEP("") GN_STEP("") GN_ARGS);
}

DI void glds4x(u32 m0, u32 v0, u32 v1, u32x4 srd, u32 soff) {
  asm volatile("s_mov_b32 m0, %[m]\n\ts_nop 0\n\tbuffer_load_dwordx4 %[v0], %[srd], %[so] offen lds\n\t"
               "s_add_u32 m0, m0, 0x400\n\ts_add_u32 %[so], %[so], 0x1c00\n\tbuffer_load_dwordx4 %[v1], %[srd], %[so] offen lds\n\t"
               "s_add_u32 m0, m0, 0x400\n\ts_add_u32 %[so], %[so], 0x1c00\n\tbuffer_load_dwordx4 %[v0], %[srd], %[so] offen lds\n\t"
               "s_add_u32 m0, m0, 0x400\n\ts_add_u32 %[so], %[so], 0x1c00\n\tbuffer_load_dwordx4 %[v1], %[srd], %[so] offen lds"
               : [so] "+s"(soff) : [m] "s"(m0), [v0] "v"(v0), [v1] "v"(v1), [srd] "s"(srd) : "memory", "scc", "m0");
}

#define STAMP(i)
#define STAMPC(i)
#define STAMP_FLUSH()

#define YI0 6
#define YSP2 6
#define EXB0 102
#define EXB1 104
#define EXB2 107
#define EXB3 110
#define RFSPLIT 6
namespace es {
constexpr int Y0 = YI0;
constexpr int ystep(int s) { return YSP2 * (s - Y0) / 2; }
constexpr int yi(int n) { for (int s = Y0; s < 28; s++) if (ystep(s) == n) return s; return -1; }
constexpr bool y(int n) { return yi(n) >= 0; }
constexpr bool st(int n) { return n % 3 == 1 && n <= 19; }
constexpr bool fl(int n) { return n == 22; }
constexpr bool exa(int n) { return n == 50 || n == 53 || n == 56; }
constexpr int pc(int n) { return n < 174 ? n : n < 185 ? 174 + 2 * (n - 174) : 175 + 2 * (n - 185); }
constexpr int RF0 = 82, RFS = 3, RFL = RF0 + 11 * RFS;
constexpr bool rf(int n) { return n >= RF0 && n <= RFL && (n - RF0) % RFS == 0; }
constexpr int WDS[24] = {65, 67, 69, 71, 73, 75, 77, 79, 81, 83, 117, 119, 121, 123, 156, 158, 160, 162, 164, 166, 169, 171, 173, 175};
constexpr int wdi(int n) { for (int i = 0; i < WD_E; i++) if (WDS[i] == n) return i; return -1; }
constexpr bool wd(int n) { return wdi(n) >= 0; }
constexpr int EXB[4] = {EXB0, EXB1, EXB2, EXB3};
constexpr int exbi(int n) { for (int i = 0; i < 4; i++) if (EXB[i] == n) return i; return -1; }
constexpr bool exb(int n) { return exbi(n) >= 0; }
static_assert(WD_E <= 24, "");
constexpr bool sta(int n) { return n == 93 || n == 97 || n == 101; }
constexpr int FA = 128, FH = 154, FB = 178, HA0 = 178;
constexpr bool stb(int n) { return n == 149 || n == 153; }
constexpr int ops(int n) { return y(n) + st(n) + fl(n) + exa(n) + rf(n) + wd(n) + exb(n) + sta(n) + (n == FA) + stb(n) + (n == FH); }
constexpr int between(int a, int b) { int c = 0; for (int n = a + 1; n < b; n++) c += ops(n); return c; }
}
namespace bs {
constexpr int cnt(int be) { return be >= 8 ? 0 : be < 8 - BP ? 2 : 1; }
constexpr int upto(int be) { int c = BP; for (int i = 0; i < be; i++) c += cnt(i); return c; }
static_assert(upto(8) == 16, "");
constexpr int younger(int be) { const int issued = 12 + upto(be) + (cnt(be) > 0 ? 1 : 0), need = 2 * be + 6; return issued - need; }
static_assert(BP != 0 || (younger(1) == 7 && younger(7) == 7 && younger(9) == 4 && younger(11) == 0), "");
}
DI void poll_flags(char* ws, u32x4 srd, u32 off, u32 tag) {
  u32 t, left = SPIN_MAX;
  asm volatile(".Lpl%=:\n\tbuffer_load_dword %[t], %[vo], %[srd], 0 offen sc1\n\ts_waitcnt vmcnt(0)\n\tv_cmp_ne_u32 vcc, %[t], %[tag]\n\ts_cbranch_vccz .Lpd%=\n\t"
               "s_sleep 1\n\ts_sub_u32 %[n], %[n], 1\n\ts_cmp_lg_u32 %[n], 0\n\ts_cbranch_scc1 .Lpl%=\n.Lpd%=:"
               : [t] "=&v"(t), [n] "+s"(left) : [vo] "v"(off), [srd] "s"(srd), [tag] "s"(tag) : "memory", "vcc", "scc");
  if (left == 0) __hip_atomic_store((AS1 u32*)(ws + WS_TMO), tag, RLX_AGENT);
}
DI void set_flag(char* ws, u32 off, u32 tag) { __hip_atomic_store((AS1 u32*)(ws + off), tag, RLX_AGENT); }
DI u32 get_granule(char* ws, u32 off, u32 tag) {
  u32 spins = 0; u64 a;
#pragma unroll 1
  for (;;) {
    a = __hip_atomic_load((AS1 u64*)(ws + off), RLX_AGENT);
    if ((u32)a == tag) break;
    if (++spins > SPIN_MAX) { set_flag(ws, WS_TMO, tag); break; }
    __builtin_amdgcn_s_sleep(1);
  }
  return (u32)(a >> 32);
}

DI void hedgehog(const f32x16& p, u32 hf, u32x4 (&fr)[4]) {
  constexpr float LG = 1.4426950408889634f;
  const float NINF = -__builtin_inff();
  float pp[16], pn[16];
#pragma unroll
  for (int r = 0; r < 16; r++) { pp[r] = p[r]; pn[r] = p[r]; }
#pragma unroll
  for (int r = 12; r < 16; r++) { pp[r] = hf ? NINF : p[r]; pn[r] = hf ? -NINF : p[r]; }
  float mx = pp[0], mn = pn[0];
#pragma unroll
  for (int r = 1; r < 16; r++) { mx = fmaxf(mx, pp[r]); mn = fminf(mn, pn[r]); }
  mx = swmax(mx); mn = swmin(mn);
  const float a = -mx * LG, b = mn * LG;
  f32x2 t0 = {0.f, 0.f}, t1 = {0.f, 0.f};
#pragma unroll
  for (int r = 0; r < 16; r += 2) {
    const f32x2 u = __builtin_elementwise_fma(f32x2{pp[r], pp[r + 1]}, f32x2{LG, LG}, f32x2{a, a});
    const f32x2 v = __builtin_elementwise_fma(f32x2{pn[r], pn[r + 1]}, f32x2{-LG, -LG}, f32x2{b, b});
    pp[r] = __builtin_amdgcn_exp2f(u[0]); pp[r + 1] = __builtin_amdgcn_exp2f(u[1]);
    pn[r] = __builtin_amdgcn_exp2f(v[0]); pn[r + 1] = __builtin_amdgcn_exp2f(v[1]);
    t0 += f32x2{pp[r], pp[r + 1]}; t1 += f32x2{pn[r], pn[r + 1]};
  }
  const float s0 = __builtin_amdgcn_rcpf(swsum(t0[0] + t0[1])), s1 = __builtin_amdgcn_rcpf(swsum(t1[0] + t1[1]));
#pragma unroll
  for (int k = 0; k < 2; k++) {
    fr[k] = u32x4{cvtpk(pp[8 * k] * s0, pp[8 * k + 1] * s0), cvtpk(pp[8 * k + 2] * s0, pp[8 * k + 3] * s0), cvtpk(pp[8 * k + 4] * s0, pp[8 * k + 5] * s0), cvtpk(pp[8 * k + 6] * s0, pp[8 * k + 7] * s0)};
    fr[2 + k] = u32x4{cvtpk(pn[8 * k] * s1, pn[8 * k + 1] * s1), cvtpk(pn[8 * k + 2] * s1, pn[8 * k + 3] * s1), cvtpk(pn[8 * k + 4] * s1, pn[8 * k + 5] * s1), cvtpk(pn[8 * k + 6] * s1, pn[8 * k + 7] * s1)};
  }
}
DI u32x4 pack8s(const f32x16& c, int o, float s) {
  return u32x4{cvtpk(c[o] * s, c[o + 1] * s), cvtpk(c[o + 2] * s, c[o + 3] * s), cvtpk(c[o + 4] * s, c[o + 5] * s), cvtpk(c[o + 6] * s, c[o + 7] * s)};
}
DI u32x4 ln_operand(float m, float ir, u32 hf) {
  const u32 h2 = cvtpk(-m, ir), l2 = cvtpk(-m - blo(h2), ir - bhi(h2));
  return hf ? u32x4{0, 0, 0, 0} : u32x4{(h2 & 0xffffu) | (l2 << 16), (h2 >> 16) | (l2 & 0xffff0000u), 0u, 0u};
}

__global__ __launch_bounds__(256, 1) void fused_block(const u16* __restrict__ x, const u16* __restrict__ wqkv, const u16* __restrict__ wphi, const u16* __restrict__ wo,
                                                      const u16* __restrict__ wg, const u16* __restrict__ wu, const u16* __restrict__ wd, const u16* __restrict__ cvec,
                                                      const u16* __restrict__ wmod, u16* __restrict__ out, char* __restrict__ ws) {
  extern __shared__ __attribute__((aligned(16))) char lds_[];
  AS3 char* L = (AS3 char*)lds_;
  const u32 LB = (u32)(size_t)L;
#define LR128(off) (*(AS3 u32x4*)(L + (off)))
#define LR64(off) (*(AS3 u32x2*)(L + (off)))
#define LR32(off) (*(AS3 u32*)(L + (off)))
#define TRR(off) __builtin_bit_cast(u32x2, __builtin_amdgcn_ds_read_tr16_b64_v4i16((AS3 s16x4*)(L + (off))))
#define BASES(name, expr) u32 name[3] = {(expr), (expr) + 65536, (expr) + 131072}; asm volatile("" : "+v"(name[0]), "+v"(name[1]), "+v"(name[2]))
#define AT(name, off) (name[(off) >> 16] + ((off) & 65535))
  const u32 w = __builtin_amdgcn_readfirstlane(threadIdx.x >> 6);
  const u32 bid = blockIdx.x;
  const u32 xh = bid & 1, mt = ((bid & 7) >> 1) * 4 + ((bid >> 3) & 3), jj = bid >> 5;
  const u32 j = xh * 8 + (((xh ? 0x76425310u : 0x75316420u) >> (jj * 4)) & 7);
  u32 lane, tid, l31, hf, tq, tp_, tblk;
#define RELANE() do { lane = __builtin_amdgcn_mbcnt_hi(~0u, __builtin_amdgcn_mbcnt_lo(~0u, 0u)); asm volatile("" : "+v"(lane)); __builtin_assume(lane < 64); \
    tid = w * 64 + lane; l31 = lane & 31; hf = lane >> 5; tq = (lane & 15) >> 2; tp_ = lane & 3; tblk = (lane >> 4) & 1; } while (0)
  RELANE();
  const u32x4 s_ws = mksrd(ws, WS_END);
  const auto r_ws = RSRC(ws, WS_END);
  STAMP(0);

  const u32 g8 = bid >> 3, x8 = bid & 7;
  const u32 gg = x8 < 4 ? (g8 < 4 ? g8 * 4 + x8 : (g8 >= 16 ? (g8 - 12) * 4 + x8 : 255)) : (g8 == 19 ? 76 + x8 : 255);
  const bool gemv = gg < 84;
  u32 vep, vc;
  u32x4 wm[7];
  {
    const u32x4 s_c = mksrd(cvec, 896), s_wm = mksrd(wmod, 448 * 5376);
    const u32 vo = (w * 112 + (lane >> 2)) * 5376 + gg * 64 + (lane & 3) * 16;
    asm volatile(
        "buffer_load_dword %[ep], off, %[sws], 0 sc1\n\t"
        "s_cmp_ge_u32 %[g], 84\n\t"
        "s_cbranch_scc1 .Lnogemv%=\n\t"
        "buffer_load_dword %[c], %[vc], %[sc], 0 offen\n\t"
        "buffer_load_dwordx4 %[w0], %[vo], %[sm], 0 offen\n\t"
        "buffer_load_dwordx4 %[w1], %[vo], %[sm], %[o1] offen\n\t"
        "buffer_load_dwordx4 %[w2], %[vo], %[sm], %[o2] offen\n\t"
        "buffer_load_dwordx4 %[w3], %[vo], %[sm], %[o3] offen\n\t"
        "buffer_load_dwordx4 %[w4], %[vo], %[sm], %[o4] offen\n\t"
        "buffer_load_dwordx4 %[w5], %[vo], %[sm], %[o5] offen\n\t"
        "buffer_load_dwordx4 %[w6], %[vo], %[sm], %[o6] offen\n"
        ".Lnogemv%=:"
        : [ep] "=&v"(vep), [c] "=&v"(vc), [w0] "=&v"(wm[0]), [w1] "=&v"(wm[1]), [w2] "=&v"(wm[2]), [w3] "=&v"(wm[3]), [w4] "=&v"(wm[4]), [w5] "=&v"(wm[5]), [w6] "=&v"(wm[6])
        : [g] "s"(gg), [vc] "v"(w * 224 + lane * 4), [vo] "v"(vo), [sc] "s"(s_c), [sm] "s"(s_wm), [sws] "s"(s_ws), [o1] "s"(86016u), [o2] "s"(2 * 86016u), [o3] "s"(3 * 86016u),
          [o4] "s"(4 * 86016u), [o5] "s"(5 * 86016u), [o6] "s"(6 * 86016u)
        : "memory", "scc");
  }
  const u32x4 s_x0 = mksrd(x, 2048 * 896);
  const u32 xve = (lane >> 3) * 896 + (((lane & 7) ^ (lane >> 4)) << 4), xvo = xve ^ 64;
  const u32 xso = (mt * 128 + w * 32) * 896, xm = LB + L_XR + w * XRW;
#define X_LD(b) glds4x(xm + (b) * 4096, xve, xvo, s_x0, xso + (b) * 128)
  X_LD(0); X_LD(1);
  {
    const u64 pc = __builtin_amdgcn_s_getpc();
    const u32 piece = ((bid >> 3) * 4 + w) % CODE_KB;
    asm volatile("s_mov_b32 m0, %0\n\ts_nop 0\n\tglobal_load_lds_dwordx4 %1, off" ::"s"(LB + LDS_BYTES - 1024), "v"((pc & ~(u64)1023) + piece * 1024 + lane * 16) : "memory", "m0");
  }
  STAMP(1);
  const bool urgent = gg < 28;
  if (!urgent) { X_LD(2); X_LD(3); X_LD(4); X_LD(5); X_LD(6); }
  if (gemv) {
    if (urgent) asm volatile("s_waitcnt vmcnt(16)" ::: "memory"); else asm volatile("s_waitcnt vmcnt(36)" ::: "memory");
    asm volatile("" : "+v"(vep), "+v"(vc));
    const u32 tag0 = __builtin_amdgcn_readfirstlane(vep) * 5u + 0x3C6EF35Fu;
    const float c0 = blo(vc), c1 = bhi(vc);
    const float si0 = c0 * __builtin_amdgcn_rcpf(1.f + __builtin_amdgcn_exp2f(-1.4426950408889634f * c0));
    const float si1 = c1 * __builtin_amdgcn_rcpf(1.f + __builtin_amdgcn_exp2f(-1.4426950408889634f * c1));
    const u32 sp = cvtpk(si0, si1);
    float a[8] = {};
    u32 sl7[7];
#pragma unroll
    for (int i = 0; i < 7; i++) sl7[i] = (u32)__builtin_amdgcn_ds_bpermute((int)((i * 8 + (lane >> 3)) * 4), (int)sp);
#pragma unroll
    for (int i = 0; i < 7; i++) sl7[i] = (lane & 4) ? sl7[i] >> 16 : sl7[i] & 0xffffu;
    asm volatile("" : "+v"(sl7[0]), "+v"(sl7[1]), "+v"(sl7[2]), "+v"(sl7[3]), "+v"(sl7[4]), "+v"(sl7[5]), "+v"(sl7[6]));
    if (urgent) asm volatile("s_waitcnt vmcnt(9)" ::: "memory"); else asm volatile("s_waitcnt vmcnt(29)" ::: "memory");
    asm volatile("" : "+v"(wm[0]), "+v"(wm[1]), "+v"(wm[2]), "+v"(wm[3]), "+v"(wm[4]), "+v"(wm[5]), "+v"(wm[6]));
#pragma unroll
    for (int i = 0; i < 7; i++) {
      const u32 sl = sl7[i], sh = sl << 16;
#pragma unroll
      for (int d = 0; d < 4; d++) { a[2 * d] = dot2(wm[i][d], sl, a[2 * d]); a[2 * d + 1] = dot2(wm[i][d], sh, a[2 * d + 1]); }
    }
    float b[4], c[2];
#pragma unroll
    for (int i = 0; i < 4; i++) { auto r = __builtin_amdgcn_permlane32_swap(asu(a[i]), asu(a[i + 4]), false, false); b[i] = asf(r[0]) + asf(r[1]); }
#pragma unroll
    for (int i = 0; i < 2; i++) {
      auto r = __builtin_amdgcn_permlane16_swap(asu(b[i]), asu(b[i + 2]), false, false);
      float v = asf(r[0]) + asf(r[1]);
      v += asf(__builtin_amdgcn_update_dpp(0, asu(v), 0x128, 0xf, 0xf, true));
      v += asf(__builtin_amdgcn_update_dpp(0, asu(v), 0x124, 0xf, 0xf, true));
      c[i] = v;
    }
    const u32 p = lane >> 4;
    if ((lane & 12) == 0) *(AS3 f32x2*)(L + L_RED + w * 128 + ((lane & 3) * 4 + (p & 1) + 2 * (p >> 1)) * 8) = f32x2{c[0], c[1]};
    BAR();
    if (tid < 16) {
      f32x2 t = *(AS3 f32x2*)(L + L_RED + tid * 8);
#pragma unroll
      for (int i = 1; i < 4; i++) { f32x2 u = *(AS3 f32x2*)(L + L_RED + i * 128 + tid * 8); t += u; }
      const u64 g = ((u64)cvtpk(t[0], t[1]) << 32) | tag0;
      __hip_atomic_store((AS1 u64*)(ws + WS_MODG + (gg * 16 + tid) * 8), g, RLX_AGENT);
    }
  }
  STAMP(2);
  if (urgent) { X_LD(2); X_LD(3); X_LD(4); X_LD(5); X_LD(6); }
  const u32x4 s_w = mksrd(wqkv, 448 * 2688);
  const u32 winb = j * 56 - (j & 1) * 8;
  u32 vw;
  {
    const u32 ci = w * 64 + lane, row = (ci * 43691) >> 19, ch = ci - row * 12;
    vw = row * 2688 + (ch >> 2) * 896 + (ch & 3) * 16;
  }
#define W_LD(g) glds(LB + ((g) < 12 ? L_W1 + (g) * 3072 + w * 1024 : L_XR + ((g) - 12) * 1024 + w * XRW), vw, s_w, (g) * 43008 + winb)
  u32x2 gsa[4], gca[4];
  const u32 gofs = WS_MODG + lane * 8;
#define G_LD() asm volatile("buffer_load_dwordx2 %0, %8, %10, 0 offen sc1\n\tbuffer_load_dwordx2 %1, %8, %10, 0 offen offset:512 sc1\n\t" \
                            "buffer_load_dwordx2 %2, %8, %10, 0 offen offset:1024 sc1\n\tbuffer_load_dwordx2 %3, %9, %10, 0 offen offset:1536 sc1\n\t" \
                            "buffer_load_dwordx2 %4, %8, %10, 0 offen offset:1792 sc1\n\tbuffer_load_dwordx2 %5, %8, %10, 0 offen offset:2304 sc1\n\t" \
                            "buffer_load_dwordx2 %6, %8, %10, 0 offen offset:2816 sc1\n\tbuffer_load_dwordx2 %7, %9, %10, 0 offen offset:3328 sc1" \
                            : "=&v"(gsa[0]), "=&v"(gsa[1]), "=&v"(gsa[2]), "=&v"(gsa[3]), "=&v"(gca[0]), "=&v"(gca[1]), "=&v"(gca[2]), "=&v"(gca[3]) \
                            : "v"(gofs), "v"(gofs - hf * 256), "s"(s_ws) : "memory")
#define WAITV2(n012, n3) asm volatile("s_cmp_eq_u32 %0, 3\n\ts_cbranch_scc1 .Lw3%=\n\ts_waitcnt vmcnt(%1)\n\ts_branch .Lwe%=\n.Lw3%=:\n\ts_waitcnt vmcnt(%2)\n.Lwe%=:" \
                                      ::"s"(w), "n"(n012), "n"(n3) : "memory", "scc")

  const u32 njob = bid >= 176 ? 2 : 1;
  const bool gu0 = bid < 224;
  const u32 pv1 = lane < 56 ? lane * 16 : OOB;
  u32 vdm = w < 3 ? vw : (gu0 ? (lane < 56 ? (lane >= 28 ? 3584 - 448 : 0) + lane * 16 : OOB) : pv1);
  const u64 pb1 = w < 3 ? (u64)wqkv : (gu0 ? (u64)wg : (u64)wd), pb2 = (w == 3 && gu0) ? (u64)wu : pb1;
  u32 sl1 = (u32)pb1, sh1 = (u32)(pb1 >> 32) & 0xffffu, sl2 = (u32)pb2, sh2 = (u32)(pb2 >> 32) & 0xffffu;
  u32 ssz = w < 3 ? 448 * 2688 : (gu0 ? 448 * 3584 : 1792 * 896);
  u32 mdm = LB + L_XR + (w < 3 ? w * XRW + BP * 1024 : 16384), odm = w < 3 ? (12 + BP) * 43008 + winb : (gu0 ? (bid >> 3) * 16 * 3584 + (bid & 7) * 448 : (bid - 224) * 16 * 896);
  u32 mdy = w < 3 ? 1024 : XRW - 7 * 1024;
  u32 ods = w < 3 ? 43008 : (gu0 ? 7168 : 896), odx = w < 3 ? 43008 : (gu0 ? 0u - 7 * 7168 : 896);
#define B_REQ(q) do { glds(mdm, vdm, (q) < 8 ? u32x4{sl1, sh1, ssz, 0x00020000u} : u32x4{sl2, sh2, ssz, 0x00020000u}, odm); \
    mdm += (q) == 7 ? mdy : 1024u; odm += (q) == 7 ? odx : ods; } while (0)
#define B_WAIT(n) asm volatile("s_cmp_eq_u32 %0, 3\n\ts_cbranch_scc1 .Lbw%=\n\ts_waitcnt vmcnt(%1)\n.Lbw%=:\n\ts_barrier" ::"s"(w), "n"(n) : "memory", "scc")
  asm volatile("" : "+s"(mdm), "+s"(odm), "+s"(sl1), "+s"(sh1), "+s"(sl2), "+s"(sh2), "+s"(ssz), "+s"(mdy), "+s"(ods), "+s"(odx), "+v"(vdm));
  u32 mk[2][4];
  {
    const u32 e = l31 & 7, on = ((l31 >> 3) & 1) == hf ? ((e & 1) ? 0xffff0000u : 0xffffu) : 0u;
#pragma unroll
    for (int c = 0; c < 2; c++)
#pragma unroll
      for (int i = 0; i < 4; i++) mk[c][i] = ((l31 >> 4) == (u32)c && (e >> 1) == (u32)i) ? on : 0u;
  }
  asm volatile("" : "+v"(mk[0][0]), "+v"(mk[0][1]), "+v"(mk[0][2]), "+v"(mk[0][3]), "+v"(mk[1][0]), "+v"(mk[1][1]), "+v"(mk[1][2]), "+v"(mk[1][3]));
  u32 wlo = L_W1 + (4 * hf + tq) * 192 + tblk * 32 + tp_ * 8, whi[3][2];
#pragma unroll
  for (int m = 0; m < 3; m++)
#pragma unroll
    for (int u = 0; u < 2; u++) { const u32 o = (4 * hf + tq + 8 * u) * 192 + m * 64 + tblk * 32 + tp_ * 8; whi[m][u] = L_XR + (o >> 10) * XRW + (o & 1023); }
  u32 tb = L_TAB + l31 * 16;
  asm volatile("" : "+v"(wlo), "+v"(whi[0][0]), "+v"(whi[0][1]), "+v"(whi[1][0]), "+v"(whi[1][1]), "+v"(whi[2][0]), "+v"(whi[2][1]), "+v"(tb));

  u32x4 xf[28];
  float rs;
  u32x4 bx1;
  {
    const u32 sw = (l31 >> 1) & 7, b0 = L_XR + w * XRW + (l31 >> 3) * 1024 + (l31 & 7) * 128;
    u32 xa[4];
#pragma unroll
    for (int m = 0; m < 4; m++) xa[m] = b0 + (((2 * m + hf) ^ sw) << 4);
    float su[2] = {0.f, 0.f}, sq[2] = {0.f, 0.f};
#define X_STAT(b) do { _Pragma("unroll") for (int m = 0; m < 4; m++) _Pragma("unroll") for (int i = 0; i < 4; i++) { \
      su[i & 1] = dot2(xf[(b) * 4 + m][i], 0x3F803F80u, su[i & 1]); sq[i & 1] = dot2(xf[(b) * 4 + m][i], xf[(b) * 4 + m][i], sq[i & 1]); } \
      asm volatile("" : "+v"(su[0]), "+v"(su[1]), "+v"(sq[0]), "+v"(sq[1])); } while (0)
    sfor<7>(LAM(b) {
      constexpr int b = b_.value;
      constexpr int nx = b == 0 ? 25 : b == 1 ? 21 : (6 - b) * 4;
      WAITV2(nx + 2 * b, nx + (b == 0 ? 0 : b <= GB ? 2 : 10));
      if constexpr (b == 0) asm volatile("" : "+v"(vep));
      if constexpr (b == 0) { STAMP(3); }
#pragma unroll
      for (int m = 0; m < 4; m++) xf[b * 4 + m] = LR128(xa[m] + b * 4096);
      if constexpr (b < 6 || BP >= 2) {
        if (w < 3) { W_LD(2 * b); W_LD(2 * b + 1); }
        else if constexpr (b == 0) {
          const u32x4 s_ph = mksrd(wphi, 16 * 1568);
          glds(LB + L_PHI, j * 1568 + lane * 16, s_ph, 0);
          glds(LB + L_PHI + 1024, j * 1568 + lane * 16 + 1024, s_ph, 0);
        } else if constexpr (b == GB) G_LD();
      }
      GAP();
      if constexpr (b > 0) X_STAT(b - 1);
      GAP();
    });
    X_STAT(6);
    STAMP(4);
    const float m = swsum(su[0] + su[1]) * (1.f / 448.f), e2 = swsum(sq[0] + sq[1]) * (1.f / 448.f);
    const float var = fmaxf(e2 - m * m, 0.f) + 1e-5f;
    rs = __builtin_amdgcn_rsqf(var);
    bx1 = ln_operand(m, var * rs, hf);
  }
  const u32 tag = __builtin_amdgcn_readfirstlane(vep) * 5u + 0x3C6EF35Fu;
  STAMP(5);
  RELANE();
  if (w < 3) sfor<(BP > 2 ? BP - 2 : 0)>(LAM(q) { constexpr int q = q_.value; W_LD(14 + q); });
  asm volatile("" : "+v"(bx1), "+v"(rs));
  STAMP(25);
  if (w == 3) {
    asm volatile("s_waitcnt vmcnt(0)" : "+v"(gsa[0]), "+v"(gsa[1]), "+v"(gsa[2]), "+v"(gsa[3]), "+v"(gca[0]), "+v"(gca[1]), "+v"(gca[2]), "+v"(gca[3])::"memory");
    u32 spins = 0;
#pragma unroll 1
    for (;;) {
      bool ok = true;
#pragma unroll
      for (int q = 0; q < 4; q++) ok = ok && gsa[q][0] == tag && gca[q][0] == tag;
      if (__all(ok)) break;
      if (++spins > SPIN_MAX) { set_flag(ws, WS_TMO, tag); break; }
      G_LD();
      asm volatile("s_waitcnt vmcnt(0)" : "+v"(gsa[0]), "+v"(gsa[1]), "+v"(gsa[2]), "+v"(gsa[3]), "+v"(gca[0]), "+v"(gca[1]), "+v"(gca[2]), "+v"(gca[3])::"memory");
    }
    STAMP(26);
#pragma unroll
    for (int q = 0; q < 4; q++) {
      if (q < 3 || lane < 32) {
        const u32 sa = gsa[q][1], sp = cvtpk(1.f + blo(gca[q][1]), 1.f + bhi(gca[q][1]));
        const u32 ad = L_TAB + (q * 64 + lane) * 32;
        *(AS3 u32x4*)(L + ad) = u32x4{__builtin_amdgcn_perm(sp, sp, 0x01000100u), __builtin_amdgcn_perm(sa, sa, 0x01000100u), 0, 0};
        *(AS3 u32x4*)(L + ad + 16) = u32x4{__builtin_amdgcn_perm(sp, sp, 0x03020302u), __builtin_amdgcn_perm(sa, sa, 0x03020302u), 0, 0};
      }
    }
    BAR();
  } else {
    WAITV(8 + BP);
    STAMP(26);
    BAR();
  }
  STAMP(6); STAMPC(40);

  f32x16 aq = {}, ak = {}, av = {};
  {
    u32x2 wr[6][2];
    u32x4 tab[2], dg[2], hb[2][2];
#define W_AD(i, u) ((i) < 36 ? wlo + ((i) / 3) * 3072 + ((i) % 3) * 64 + (u) * 1536 : whi[(i) % 3][u] + ((i) / 3 - 12) * 1024)
#define W_RD(i) do { wr[(i) % 6][0] = TRR(W_AD(i, 0)); wr[(i) % 6][1] = TRR(W_AD(i, 1)); } while (0)
#define DIAG(c, t) u32x4{(t)[0] & mk[c][0], (t)[0] & mk[c][1], (t)[0] & mk[c][2], (t)[0] & mk[c][3]}
    tab[0] = LR128(tb); tab[1] = LR128(tb + 512);
    W_RD(0); W_RD(1); W_RD(2);
    {
      f32x16 h0 = mfma(DIAG(0, tab[0]), xf[0], f32x16{});
      h0 = mfma(DIAG(1, tab[0]), xf[1], h0);
      h0 = mfma(tab[0], bx1, h0);
      hb[0][0] = pack8(h0, 0); hb[0][1] = pack8(h0, 8);
    }
    dg[0] = DIAG(0, tab[1]);
    asm volatile("" : "+v"(dg[0]));
    GAP();
    f32x16 hh;
    sfor<14>(LAM(be) {
      constexpr int be = be_.value, i0 = 6 * be, cur = be & 1, nx = cur ^ 1;
      constexpr bool more = be < 13;
      if constexpr (more) {
        asm volatile("v_mfma_f32_32x32x16_bf16 %0, %1, %2, 0" : "=&v"(hh) : "v"(dg[0]), "v"(xf[2 * be + 2]));
        dg[1] = DIAG(1, tab[nx]);
        asm volatile("" : "+v"(dg[1]));
      }
      GAP();
      aq = mfma(cat(wr[(i0 + 0) % 6][0], wr[(i0 + 0) % 6][1]), hb[cur][0], aq);
      W_RD(i0 + 3);
      if constexpr (be < 12) tab[cur] = LR128(tb + (be + 2) * 512);
      GAP();
      if constexpr (more) asm volatile("v_mfma_f32_32x32x16_bf16 %0, %1, %2, %0" : "+v"(hh) : "v"(dg[1]), "v"(xf[2 * be + 3]));
      W_RD(i0 + 4);
      if constexpr (be < 8) B_REQ(2 * be);
      GAP();
      ak = mfma(cat(wr[(i0 + 1) % 6][0], wr[(i0 + 1) % 6][1]), hb[cur][0], ak);
      W_RD(i0 + 5);
      GAP();
      if constexpr (more) asm volatile("v_mfma_f32_32x32x16_bf16 %0, %1, %2, %0" : "+v"(hh) : "v"(tab[nx]), "v"(bx1));
      if constexpr ((be & 1) && be < 12) B_WAIT(bs::younger(be));
      if constexpr (more) W_RD(i0 + 6);
      GAP();
      av = mfma(cat(wr[(i0 + 2) % 6][0], wr[(i0 + 2) % 6][1]), hb[cur][0], av);
      if constexpr (more) W_RD(i0 + 7);
      if constexpr (be >= 8 && be < 12) {
        if (njob == 2 && w == 3) glds4<0>(LB + L_XR + (be < 10 ? 3 * XRW + (be - 8) * 4096 : (be - 10) * XRW + 24576), pv1, mksrd(wd, 1792 * 896), ((bid - 144) * 16 + (be - 8) * 4) * 896, 1024, 896);
      }
      if constexpr (be < 12) { dg[0][0] = tab[cur][0] & mk[0][0]; dg[0][1] = tab[cur][0] & mk[0][1]; asm volatile("" : "+v"(dg[0][0]), "+v"(dg[0][1])); }
      GAP();
      aq = mfma(cat(wr[(i0 + 3) % 6][0], wr[(i0 + 3) % 6][1]), hb[cur][1], aq);
      if constexpr (more) W_RD(i0 + 8);
      if constexpr (be < 12) { dg[0][2] = tab[cur][0] & mk[0][2]; dg[0][3] = tab[cur][0] & mk[0][3]; asm volatile("" : "+v"(dg[0][2]), "+v"(dg[0][3])); }
      GAP();
      ak = mfma(cat(wr[(i0 + 4) % 6][0], wr[(i0 + 4) % 6][1]), hb[cur][1], ak);
      if constexpr (more) {
        asm volatile("" : "+v"(hh));
        hb[nx][0] = pack8(hh, 0);
        asm volatile("" : "+v"(hb[nx][0]));
      }
      if constexpr (be < 8) { if constexpr (bs::cnt(be) == 2) B_REQ(2 * be + 1); else if (w == 3) B_REQ(2 * be + 1); }
      GAP();
      av = mfma(cat(wr[(i0 + 5) % 6][0], wr[(i0 + 5) % 6][1]), hb[cur][1], av);
      if constexpr (more) { hb[nx][1] = pack8(hh, 8); asm volatile("" : "+v"(hb[nx][1])); }
      GAP();
    });
  }
  RELANE();
  const u32 odd = j & 1;
  u32x4 aph[2];
  {
#pragma unroll
    for (int s = 0; s < 2; s++) {
      u32x2 t[2];
#pragma unroll
      for (int u = 0; u < 2; u++) {
        int d = 16 * s + 8 * u + 4 * (int)hf + (int)tq - 4 * (int)odd;
        d = d < 0 ? 0 : (d > 27 ? 27 : d);
        t[u] = TRR(L_PHI + d * 56 + tblk * 32 + tp_ * 8);
      }
      aph[s] = cat(t[0], t[1]);
    }
    const bool zl = odd && !hf, zh = !odd && hf;
    aph[0][0] = zl ? 0 : aph[0][0]; aph[0][1] = zl ? 0 : aph[0][1]; aph[1][2] = zh ? 0 : aph[1][2]; aph[1][3] = zh ? 0 : aph[1][3];
  }
  BAR();
  STAMP(7); STAMPC(41);
  RELANE();
  const bool dwg = jj < 7;
  const u32 tph = xh, ng = jj;
  {
    f32x16 pk = {};
    pk = mfma(aph[0], pack8s(ak, 0, rs), pk);
    pk = mfma(aph[1], pack8s(ak, 8, rs), pk);
    u32x4 v0 = pack8s(av, 0, rs), v1 = pack8s(av, 8, rs);
    const bool zl = odd && !hf, zh = !odd && hf;
    v0[0] = zl ? 0x3F80u : v0[0]; v0[1] = zl ? 0 : v0[1]; v1[2] = zh ? 0x3F80u : v1[2]; v1[3] = zh ? 0 : v1[3];
    const u32 t = w * 32 + l31;
    const u32 va = (L_VIM + t * 64) | ((hf ^ ((l31 >> 1) & 7)) << 3);
    *(AS3 u32x2*)(L + va) = u32x2{v0[0], v0[1]};
    *(AS3 u32x2*)(L + (va ^ 16)) = u32x2{v0[2], v0[3]};
    *(AS3 u32x2*)(L + (va ^ 32)) = u32x2{v1[0], v1[1]};
    *(AS3 u32x2*)(L + (va ^ 48)) = u32x2{v1[2], v1[3]};
    u32x4 fk[4];
    hedgehog(pk, hf, fk);
    const u32 swp = (l31 & 7) | ((((l31 >> 3) ^ (l31 >> 1)) & 1) << 3);
    const u32 pa = (L_PHK + t * 128) | ((hf ^ swp) << 3);
#pragma unroll
    for (int s = 0; s < 4; s++) {
      const u32 u0 = (s >> 1) * 8 + (s & 1) * 4;
      *(AS3 u32x2*)(L + (pa ^ (u0 << 3))) = u32x2{fk[s][0], fk[s][1]};
      *(AS3 u32x2*)(L + (pa ^ ((u0 + 2) << 3))) = u32x2{fk[s][2], fk[s][3]};
    }
  }
  STAMP(28);
  RELANE();
  u32 a0, a1, b0, b1, kvo, kvf, kvt = tag;
  {
    const u32 swa = tq | ((hf ^ (tq >> 1)) & 1) << 3;
    const u32 ua = (w & 1) * 8 + tblk * 4 + tp_;
    a0 = L_PHK + (8 * hf + tq) * 128 + ((ua ^ swa) << 3); a1 = L_PHK + (8 * hf + 4 + tq) * 128 + ((ua ^ swa ^ 4) << 3);
    const u32 swb = 4 * hf + (tq >> 1), ub = tblk * 4 + tp_;
    b0 = L_VIM + (8 * hf + tq) * 64 + ((ub ^ swb) << 3); b1 = L_VIM + (8 * hf + 4 + tq) * 64 + ((ub ^ swb ^ 2) << 3);
    kvo = WS_KVP + ((j * 4 + w * 2) * 16 + mt) * 1024 + lane * 16;
    kvf = lane == 0 ? WS_KVF + (j * 2 + w) * 128 + mt * 4 : OOB;
    asm volatile("" : "+v"(a0), "+v"(a1), "+v"(b0), "+v"(b1), "+v"(kvo), "+v"(kvf), "+v"(kvt));
  }
  WAITV(0);
  BAR();
  STAMP(8);
  if (w < 2) {
    f32x16 kv = {}, kv1 = {};
    u32x2 xa[8][4];
#pragma unroll
    for (int kk = 0; kk < 8; kk++) { xa[kk][0] = TRR(a0 + kk * 2048); xa[kk][1] = TRR(a1 + kk * 2048); xa[kk][2] = TRR(b0 + kk * 1024); xa[kk][3] = TRR(b1 + kk * 1024); }
    GAP();
#pragma unroll
    for (int kk = 0; kk < 8; kk++) {
      if (kk & 1) kv1 = mfma(cat(xa[kk][0], xa[kk][1]), cat(xa[kk][2], xa[kk][3]), kv1);
      else kv = mfma(cat(xa[kk][0], xa[kk][1]), cat(xa[kk][2], xa[kk][3]), kv);
    }
    kv += kv1;
    __builtin_amdgcn_raw_buffer_store_b128(pack8(kv, 0), r_ws, kvo, 0, 16);
    __builtin_amdgcn_raw_buffer_store_b128(pack8(kv, 8), r_ws, kvo, 16384, 16);
    WAITV(0);
    aflag<0>(kvt, kvf, s_ws);
  }
  STAMP(9);
  RELANE();
  if (w >= 2) {
#pragma unroll 1
    for (u32 jb = 0; jb < njob; jb++) {
      const u32 id = bid + jb * 80;
      const u32 rowl = 4 * hf + tq;
      u32 ad, st, go, gd;
      if (id < 224) {
        ad = L_XR + 16384 + tblk * XRW + (rowl >> 1) * 1024 + (rowl & 1) * 448 + (w - 2) * 224 + tp_ * 8; st = 4096;
        go = WS_WGU + ((id & 7) * 2 + (w - 2)) * 196 * 1024; gd = id >> 3;
      } else {
        ad = L_XR + (jb ? 3 * XRW : 16384) + rowl * 1024 + (w - 2) * 448 + tblk * 32 + tp_ * 8;
        st = jb ? 24576 + hf * XRW - hf * 4096 - 3 * XRW : XRW;
        go = WS_WD + ((w - 2) * 7 * 112 + (id - 224)) * 1024; gd = 0;
      }
      u32x2 t[7][2];
#pragma unroll
      for (int i = 0; i < 7; i++) { const u32 o = id < 224 ? i * 32 : i * 64; t[i][0] = TRR(ad + o); t[i][1] = TRR(ad + o + st); }
#pragma unroll
      for (int i = 0; i < 7; i++) {
        const u32 g = id < 224 ? (i < 3 ? gd * 3 + i : i < 5 ? 84 + gd * 2 + i - 3 : 140 + gd * 2 + i - 5) : i * 112;
        __builtin_amdgcn_raw_buffer_store_b128(cat(t[i][0], t[i][1]), r_ws, lane * 16, go + g * 1024, 16);
      }
    }
  }
  if (dwg && w >= 2) {
    const u32x4 s_wo = mksrd(wo, 448 * 896), s_x = mksrd(x, 2048 * 896);
    const u32 r = lane >> 3, vo = r * 896 + (((lane & 7) ^ (((r >> 1) & 1) << 2)) << 4);
    const u32 p0 = (w - 2) * 28;
#pragma unroll
    for (int i = 0; i < 4; i++) glds7<0>(LB + L_WO + (p0 + i * 7) * 1024, vo, s_wo, (p0 + i * 7) * 7168 + ng * 128, 1024, 7168);
    const u32 vx = r * 896 + (((lane & 7) ^ r) << 4);
    glds4<0>(LB + L_XRES + (w - 2) * 4096, vx, s_x, (mt * 128 + tph * 64 + (w - 2) * 32) * 896 + ng * 128, 1024, 7168);
  }
  RELANE();
  u32x4 fq[4];
  {
    f32x16 pq = {};
    pq = mfma(aph[0], pack8s(aq, 0, rs), pq);
    pq = mfma(aph[1], pack8s(aq, 8, rs), pq);
    hedgehog(pq, hf, fq);
  }
  STAMP(10);

  u32 which, gof;
  bool gact;
  u32x2 gr;
  {
    u32 vj0 = bid < 224 ? WS_PF + ((bid & 7) * 2 + (w - 2)) * 128 + (bid >> 3) * 4 : WS_PD + (w - 2) * 512 + (bid - 224) * 4, vj1 = WS_PD + (w - 2) * 512 + (bid - 144) * 4, vjt = tag;
    vj0 = w >= 2 && lane == 0 ? vj0 : OOB; vj1 = w >= 2 && lane == 0 && bid >= 176 ? vj1 : OOB;
    u32 vl16 = lane * 16, ofo = lane == 0 ? WS_OF + (mt * 4 + w) * 128 + j * 4 : OOB;
    asm volatile("" : "+v"(vj0), "+v"(vj1), "+v"(vjt), "+v"(vl16), "+v"(ofo));
    poll_flags(ws, s_ws, WS_KVF + (j * 2 + (w >> 1)) * 128 + (lane & 15) * 4, tag);
    aflag<0>(vjt, vj0, s_ws); aflag<0>(vjt, vj1, s_ws);
    STAMP(11);
    u32x4 pt[16];
    {
      const u32 pb = WS_KVP + (j * 4 + w) * 16 * 1024;
      sfor<16>(LAM(i) { constexpr int i = i_.value; aload128(pt[i], vl16, s_ws, pb + ((i + mt) & 15) * 1024); });
    }
    which = (w & 1) * 2 + hf;
    gof = WS_MODG + ((which == 0 ? 448 : which == 1 ? 896 : 1120) + ng * 32 + l31) * 8;
    gact = dwg && w >= 2 && which < 3;
    aload64(gr, gact ? gof : OOB, s_ws);
    float sm[8] = {};
    u32 one_lo = 0x00003F80u, one_hi = 0x3F800000u;
    asm("" : "+s"(one_lo), "+s"(one_hi));
    sfor<4>(LAM(g) {
      constexpr int g = g_.value;
      await4<1 + 12 - 4 * g>(pt[4 * g], pt[4 * g + 1], pt[4 * g + 2], pt[4 * g + 3]);
      if constexpr (g == 3) { STAMP(29); }
#pragma unroll
      for (int i = 4 * g; i < 4 * g + 4; i++)
#pragma unroll
        for (int e = 0; e < 4; e++) { sm[2 * e] = dot2(pt[i][e], one_lo, sm[2 * e]); sm[2 * e + 1] = dot2(pt[i][e], one_hi, sm[2 * e + 1]); }
      asm volatile("" : "+v"(sm[0]), "+v"(sm[1]), "+v"(sm[2]), "+v"(sm[3]), "+v"(sm[4]), "+v"(sm[5]), "+v"(sm[6]), "+v"(sm[7]));
    });
    *(AS3 u32x4*)(L + L_KVS + w * 1024 + lane * 16) = u32x4{cvtpk(sm[0], sm[1]), cvtpk(sm[2], sm[3]), cvtpk(sm[4], sm[5]), cvtpk(sm[6], sm[7])};
    STAMP(30);
    BAR();
    STAMP(31);
    f32x16 o = {}, o1_ = {};
#pragma unroll
    for (int s = 0; s < 2; s++) { o = mfma(LR128(L_KVS + s * 1024 + lane * 16), fq[s], o); o1_ = mfma(LR128(L_KVS + (s + 2) * 1024 + lane * 16), fq[s + 2], o1_); }
    o += o1_;
    auto dd = __builtin_amdgcn_permlane32_swap(asu(odd ? o[0] : o[12]), asu(odd ? o[0] : o[12]), false, false);
    const float inv = __builtin_amdgcn_rcpf(asf(odd ? dd[0] : dd[1]) + 1e-6f);
    u32x4 o0 = pack8s(o, 0, inv), o1 = pack8s(o, 8, inv);
    const bool zl = odd && !hf, zh = !odd && hf;
    o0[0] = zl ? 0 : o0[0]; o0[1] = zl ? 0 : o0[1]; o1[2] = zh ? 0 : o1[2]; o1[3] = zh ? 0 : o1[3];
    const u32 base = WS_OFR + (((mt * 4 + w) * 16 + j) * 2) * 1024 + lane * 16;
    __builtin_amdgcn_raw_buffer_store_b128(o0, r_ws, base, 0, 16);
    __builtin_amdgcn_raw_buffer_store_b128(o1, r_ws, base + 1024, 0, 16);
    STAMP(32);
    const u32 pfo = WS_PF + j * 128 + (l31 < 28 ? l31 : 0) * 4;
    const u32 pdb = WS_PD + (ng * 2 + (w & 1) >= 7 ? 512 : 0), pdo0 = pdb + lane * 4, pdo1 = pdb + 256 + (lane < 48 ? lane : 0) * 4;
    u32 pf, pd0, pd1;
    asm volatile("buffer_load_dword %0, %3, %6, 0 offen sc1\n\tbuffer_load_dword %1, %4, %6, 0 offen sc1\n\tbuffer_load_dword %2, %5, %6, 0 offen sc1\n\ts_waitcnt vmcnt(0)"
                 : "=&v"(pf), "=&v"(pd0), "=&v"(pd1) : "v"(pfo), "v"(pdo0), "v"(pdo1), "s"(s_ws) : "memory");
    aflag<0>(vjt, ofo, s_ws);
    STAMP(12);
    if (!__all(pf == tag && pd0 == tag && pd1 == tag)) { poll_flags(ws, s_ws, pfo, tag); poll_flags(ws, s_ws, pdo0, tag); poll_flags(ws, s_ws, pdo1, tag); }
  }
  STAMP(13);
  RELANE();
  const u32 ring_src = WS_WGU + j * 196 * 1024;
#define RING_LOAD(p0, n) sfor<n>(LAM(i) { constexpr int i = i_.value; glds1(LB + (((p0) + 4 * i) % RING) * 1024 + w * 1024, lane * 16, s_ws, ring_src + ((p0) + 4 * i) * 1024 + w * 1024); })

  const u32 nh = w & 1, th = w >> 1;
  u32x4 x1p[2];
  x1p[0] = u32x4{0, 0, 0, 0}; x1p[1] = x1p[0];
  u32x4 wdf[56];
  const u32 wd_src = WS_WD + (((dwg ? ng : 0) * 2 + nh) * 112 + th * 56) * 1024;
#define WD_KS(q) ((q) < 24 ? ((q) / 3) * 7 + (q) % 3 : (q) < 40 ? (((q) - 24) >> 1) * 7 + 3 + ((q) & 1) : (((q) - 40) >> 1) * 7 + 5 + ((q) & 1))
#define WD_LD(q, vo) wdf[q] = __builtin_amdgcn_raw_buffer_load_b128(r_ws, vo, wd_src + WD_KS(q) * 1024, 16)
  if (dwg) {
    RING_LOAD(0, PRE_A / 4);
    sfor<WD_A>(LAM(q) { constexpr int q = q_.value; WD_LD(q, lane * 16); });
    asm volatile("" : "+v"(gr));
    if (gact) {
      const u32 d = gr[0] == tag ? gr[1] : get_granule(ws, gof, tag);
      float lo = blo(d), hi = bhi(d);
      if (which == 1) { const u32 sp = cvtpk(1.f + lo, 1.f + hi); lo = blo(sp); hi = bhi(sp); }
      *(AS3 f32x2*)(L + (which == 0 ? L_GA : which == 1 ? L_S2 : L_GB) + l31 * 8) = f32x2{lo, hi};
    }
    const u32 tile = mt * 4 + tph * 2 + th;
    poll_flags(ws, s_ws, WS_OF + tile * 128 + (nh * 8 + (lane & 7)) * 4, tag);
    STAMP(14);
    u32x4 of[16];
#pragma unroll
    for (int i = 0; i < 16; i++) of[i] = __builtin_amdgcn_raw_buffer_load_b128(r_ws, lane * 16, WS_OFR + (tile * 32 + nh * 16 + i) * 1024, 16);
    f32x16 acc[2] = {};
    {
      u32 wl[2];
#pragma unroll
      for (int t = 0; t < 2; t++) wl[t] = L_WO + nh * 224 * 128 + (4 * hf + tq) * 128 + ((((t ^ nh) * 4 + tblk * 2 + (tp_ >> 1)) ^ ((tq >> 1) << 2)) << 4) + (tp_ & 1) * 8;
      asm volatile("" : "+v"(wl[0]), "+v"(wl[1]));
      u32x2 wr[6][2];
#define O_ROW(n) (28 * ((n) >> 2) - 4 * (((n) >> 2) & 1) + 16 * (((n) >> 1) & 1))
#define O_RD(n) do { wr[(n) % 6][0] = TRR(wl[(n) & 1] + O_ROW(n) * 128); wr[(n) % 6][1] = TRR(wl[(n) & 1] + (O_ROW(n) + 8) * 128); } while (0)
      O_RD(0); O_RD(1); O_RD(2); O_RD(3);
      GAP();
      sfor<32>(LAM(n) {
        constexpr int n = n_.value;
        acc[n & 1] = mfma(cat(wr[n % 6][0], wr[n % 6][1]), of[n >> 1], acc[n & 1]);
        if constexpr (n + 4 < 32) O_RD(n + 4);
        if constexpr (n >= 32 - WD_B) WD_LD(WD_A + n - (32 - WD_B), lane * 16);
        GAP();
      });
    }
    STAMP(15);
    BAR();
#define RING1(p) glds1(LB + (p) * 1024 + w * 1024, lane * 16, s_ws, ring_src + (p) * 1024 + w * 1024)
    RING1(80); RING1(84); RING1(88);
    {
      const f32x16& snd = acc[1];
      const u32 ad = L_DEX + (w ^ 1) * 4096 + lane * 16;
#pragma unroll
      for (int g = 0; g < 4; g++) *(AS3 f32x4*)(L + ad + g * 1024) = f32x4{snd[4 * g], snd[4 * g + 1], snd[4 * g + 2], snd[4 * g + 3]};
    }
    RING1(92); RING1(96);
    f32x4 ga4[4], s24[4];
    u32x2 xr4[4];
#pragma unroll
    for (int g = 0; g < 4; g++) {
      ga4[g] = *(AS3 f32x4*)(L + L_GA + (nh * 32 + g * 8 + 4 * hf) * 4); s24[g] = *(AS3 f32x4*)(L + L_S2 + (nh * 32 + g * 8 + 4 * hf) * 4);
      xr4[g] = LR64(L_XRES + (th * 32 + l31) * 128 + (((nh * 4 + g) ^ (l31 & 7)) << 4) + hf * 8);
    }
    u32 xfo = lane == 0 ? WS_XF + tile * 128 + (ng * 2 + nh) * 4 : OOB, xft = tag;
    asm volatile("" : "+v"(xfo), "+v"(xft));
    BAR();
    u32x4 yp[2];
    float su = 0.f, sq = 0.f;
#pragma unroll
    for (int g = 0; g < 4; g++) {
      const f32x4 ot = *(AS3 f32x4*)(L + L_DEX + w * 4096 + lane * 16 + g * 1024);
      const f32x4 ga = ga4[g], s2 = s24[g];
      const u32x2 xr = xr4[g];
      RING1(100 + g * 8); RING1(104 + g * 8);
      float a[4];
#pragma unroll
      for (int e = 0; e < 4; e++) a[e] = acc[0][4 * g + e] + ot[e];
      const u32 p0 = cvtpk(__builtin_fmaf(ga[0], a[0], blo(xr[0])), __builtin_fmaf(ga[1], a[1], bhi(xr[0])));
      const u32 p1 = cvtpk(__builtin_fmaf(ga[2], a[2], blo(xr[1])), __builtin_fmaf(ga[3], a[3], bhi(xr[1])));
      x1p[g >> 1][(g & 1) * 2] = p0; x1p[g >> 1][(g & 1) * 2 + 1] = p1;
      su = dot2(p0, 0x3F803F80u, su); su = dot2(p1, 0x3F803F80u, su);
      sq = dot2(p0, p0, sq); sq = dot2(p1, p1, sq);
      yp[g >> 1][(g & 1) * 2] = cvtpk(blo(p0) * s2[0], bhi(p0) * s2[1]);
      yp[g >> 1][(g & 1) * 2 + 1] = cvtpk(blo(p1) * s2[2], bhi(p1) * s2[3]);
    }
    su = swsum(su); sq = swsum(sq);
    const u32 nt = ng * 2 + nh;
    __builtin_amdgcn_raw_buffer_store_b128(yp[0], r_ws, WS_YF + (tile * 28 + nt * 2) * 1024 + lane * 16, 0, 16);
    __builtin_amdgcn_raw_buffer_store_b128(yp[1], r_ws, WS_YF + (tile * 28 + nt * 2 + 1) * 1024 + lane * 16, 0, 16);
    if (hf == 0) __builtin_amdgcn_raw_buffer_store_b64(u32x2{asu(su), asu(sq)}, r_ws, WS_STAT + (tile * 14 + nt) * 256 + l31 * 8, 0, 16);
    STAMP(33);
    WAITV(0);
    STAMP(34);
    aflag<0>(xft, xfo, s_ws);
    BAR();
    STAMP(16);
    RING_LOAD(132, 4);
    sfor<WD_C>(LAM(q) { constexpr int q = q_.value; WD_LD(WD_A + WD_B + q, lane * 16); });
    STAMP(35);
  } else {
    const u32 mat = (mt >> 1) & 1, ft = xh * 8 + (mt >> 2) * 2 + (mt & 1);
    const auto r_m = RSRC(mat ? wu : wg, 448 * 3584);
    const u32 ch = lane % 14, sub = lane / 14;
    u32x4 wv[28];
#pragma unroll
    for (int i = 0; i < 28; i++) wv[i] = __builtin_amdgcn_raw_buffer_load_b128(r_m, lane < 56 ? (i * 16 + w * 4 + sub) * 3584 + ft * 224 + ch * 16 : OOB, 0, 0);
    if (tid < 224) {
      const u32 cb = get_granule(ws, WS_MODG + (896 + tid) * 8, tag), sb = get_granule(ws, WS_MODG + (672 + tid) * 8, tag), sp = cvtpk(1.f + blo(cb), 1.f + bhi(cb));
      *(AS3 u32x4*)(L + L_P1T + tid * 32) = u32x4{sp & 0xffffu, sp << 16, sb & 0xffffu, sb << 16};
      *(AS3 u32x4*)(L + L_P1T + tid * 32 + 16) = u32x4{sp >> 16, sp & 0xffff0000u, sb >> 16, sb & 0xffff0000u};
    }
    BAR();
    float au[8] = {}, ab[8] = {};
    const u32 subc = sub < 4 ? sub : 0;
#pragma unroll
    for (int i = 0; i < 28; i++) {
      const u32x4 t = LR128(L_P1T + (i * 16 + w * 4 + subc) * 16);
#pragma unroll
      for (int e = 0; e < 4; e++) {
        au[2 * e] = dot2(wv[i][e], t[0], au[2 * e]); au[2 * e + 1] = dot2(wv[i][e], t[1], au[2 * e + 1]);
        ab[2 * e] = dot2(wv[i][e], t[2], ab[2 * e]); ab[2 * e + 1] = dot2(wv[i][e], t[3], ab[2 * e + 1]);
      }
    }
    if (lane < 56) {
      const u32 ro = L_P1R + (w * 4 + sub) * 896 + ch * 32;
      *(AS3 f32x4*)(L + ro) = f32x4{au[0], au[1], au[2], au[3]};
      *(AS3 f32x4*)(L + ro + 16) = f32x4{au[4], au[5], au[6], au[7]};
      *(AS3 f32x4*)(L + ro + 448) = f32x4{ab[0], ab[1], ab[2], ab[3]};
      *(AS3 f32x4*)(L + ro + 464) = f32x4{ab[4], ab[5], ab[6], ab[7]};
    }
    BAR();
    if (tid < 112) {
      float u = 0.f, b = 0.f;
#pragma unroll
      for (int i = 0; i < 16; i++) { u += asf(LR32(L_P1R + i * 896 + tid * 4)); b += asf(LR32(L_P1R + i * 896 + 448 + tid * 4)); }
      const u32 h2 = cvtpk(u, b);
      const u32 l2 = cvtpk(u - blo(h2), b - bhi(h2));
      const u32 uh = h2 & 0xffffu, bh = h2 >> 16, ul = l2 & 0xffffu, bl = l2 >> 16;
      __builtin_amdgcn_raw_buffer_store_b128(u32x4{uh * 0x10001u, bh * 0x10001u, ul, bl}, r_ws,
                                             WS_EXF + ((ft * 7 + (tid >> 4)) * 32 + (tid & 15) + 16 * mat) * 16, 0, 16);
    }
    WAITV(0);
    BAR();
    if (tid == 0) set_flag(ws, WS_UF + (ft * 2 + mat) * 4, tag);
    RING_LOAD(0, 37);
  }

  RELANE();
  const u32 kh = th, ltile = mt * 4 + tph * 2 + nh;
  const u32 hfo = WS_HF + ltile * 256 + (kh * 8 + (lane & 7)) * 4;
#define H_SLOT(c) (((c) & 3) == 3 ? 0 : 49152 + ((c) & 3) * 32768)
  const u32 ldst = LB + (kh * 2 + nh) * 8192, lsrc = WS_HH + (ltile * 112 + kh * 56) * 1024;
  const u32 h_vo = dwg ? lane * 16 : OOB, wd_vo = h_vo;
  u32 vtag = tag, hso = lane == 0 ? WS_HF + (mt * 4 + w) * 256 + j * 4 : OOB;
  asm volatile("" : "+v"(vtag), "+v"(hso));
#define H_LD(q) glds1(ldst + H_SLOT((q) >> 3) + ((q) & 7) * 1024, h_vo, s_ws, lsrc + WD_KS(q) * 1024)
  {
    const u32 tile = mt * 4 + w;
    if (w < 2) glds1(LB + (148 + w) * 1024, lane * 16, s_ws, ring_src + (148 + w) * 1024);
    u32x4 ex[7];
    poll_flags(ws, s_ws, WS_XF + tile * 128 + ((lane & 15) < 14 ? (lane & 15) : 0) * 4, tag);
    if (bid == 0 && tid == 0) set_flag(ws, WS_EPOCH, tag);
    STAMP(17);
    u32x4 yf[28];
#define Y_LD(s) yf[s] = __builtin_amdgcn_raw_buffer_load_b128(r_ws, lane * 16, WS_YF + (tile * 28 + (s)) * 1024, 16)
    sfor<es::Y0>(LAM(s) { constexpr int s = s_.value; Y_LD(s); });
    u32x2 st[7];
    VWAITN(es::Y0 - 1);
    RAWBAR();
    STAMP(18); STAMPC(42);
    f32x16 aA[3], aB[2], aC[2];
    u32x4 bx, hv;
    u32 fl, fh;
    const u32 flo = WS_UF + (j * 2 + (lane & 1)) * 4;
    constexpr int PD = 6, AR = 8;
    u32x4 ar[AR];
    BASES(lb, lane * 16);
#define E_RD(n) LR128(AT(lb, (es::pc(n) % RING) * 1024))
#define EX_LD(t) ex[t] = __builtin_amdgcn_raw_buffer_load_b128(r_ws, hf ? OOB : l31 * 16, WS_EXF + (j * 7 + (t)) * 512, 16)
#define HH_ST(t, v) __builtin_amdgcn_raw_buffer_store_b128(v, r_ws, lane * 16, WS_HH + (tile * 112 + j * 7 + (t)) * 1024, 16)
#define PROD(a, d) cvtpk((a)[2 * (d)] * (a)[2 * (d) + 8], (a)[2 * (d) + 1] * (a)[2 * (d) + 9])
    sfor<PD>(LAM(n) { constexpr int n = n_.value; ar[n] = E_RD(n); });
    GAP();
    float ssu = 0.f, ssq = 0.f, smean = 0.f, svar = 0.f;
    sfor<196>(LAM(n) {
      constexpr int n = n_.value;
      if constexpr (n < 84) aA[n % 3] = mfma(ar[n % AR], yf[n / 3], n < 3 ? f32x16{} : aA[n % 3]);
      else if constexpr (n < 140) aB[n & 1] = mfma(ar[n % AR], yf[(n - 84) >> 1], n < 86 ? f32x16{} : aB[n & 1]);
      else {
        constexpr int p = es::pc(n);
        aC[p & 1] = mfma(ar[n % AR], yf[(p - 140) >> 1], n < 142 ? f32x16{} : aC[p & 1]);
        if constexpr (n >= 168) keep_acc(aC[p & 1]);
      }
      if constexpr (es::y(n)) Y_LD(es::yi(n));
      if constexpr (es::st(n)) st[n / 3] = __builtin_amdgcn_raw_buffer_load_b64(r_ws, l31 * 8 + hf * 7 * 256, WS_STAT + (tile * 14 + n / 3) * 256, 16);
      if constexpr (es::fl(n)) aload32(fl, flo, s_ws);
      if constexpr (n == 46) {
        await1<es::between(22, 46)>(fl);
        if (!__all(fl == tag)) poll_flags(ws, s_ws, flo, tag);
      }
      if constexpr (es::exa(n)) EX_LD((n - 50) / 3);
      if constexpr (es::exb(n)) EX_LD(3 + es::exbi(n));
      if constexpr (es::wd(n)) WD_LD(WD_P + es::wdi(n), wd_vo);
      if constexpr (n == 52) { ssu = asf(st[0][0]) + asf(st[1][0]) + asf(st[2][0]) + asf(st[3][0]); asm volatile("" : "+v"(ssu)); }
      if constexpr (n == 54) { ssq = asf(st[0][1]) + asf(st[1][1]) + asf(st[2][1]) + asf(st[3][1]); asm volatile("" : "+v"(ssq)); }
      if constexpr (n == 55) { ssu += asf(st[4][0]) + asf(st[5][0]) + asf(st[6][0]); asm volatile("" : "+v"(ssu)); }
      if constexpr (n == 57) { ssq += asf(st[4][1]) + asf(st[5][1]) + asf(st[6][1]); asm volatile("" : "+v"(ssq)); }
      if constexpr (n == 59) { smean = swsum(ssu) * (1.f / 448.f); asm volatile("" : "+v"(smean)); }
      if constexpr (n == 61) { svar = swsum(ssq) * (1.f / 448.f); asm volatile("" : "+v"(svar)); }
      if constexpr (n == 63) { svar = fmaxf(svar - smean * smean, 0.f) + 1e-5f; ssu = __builtin_amdgcn_rsqf(svar); asm volatile("" : "+v"(svar), "+v"(ssu)); }
      if constexpr (n == 65) {
        if (hf == 0) *(AS3 float*)(L + L_R2 + (w * 32 + l31) * 4) = ssu * ssu;
        svar *= ssu;
        asm volatile("" : "+v"(svar));
      }
      if constexpr (n == 67) { bx[2] = cvtpk(-smean, svar); asm volatile("" : "+v"(bx[2])); }
      if constexpr (n == 69) { bx[3] = cvtpk(-smean - blo(bx[2]), svar - bhi(bx[2])); asm volatile("" : "+v"(bx[3])); }
      if constexpr (n == 71) {
        const u32 h2 = hf ? 0u : bx[2], l2 = hf ? 0u : bx[3];
        bx = u32x4{(h2 & 0xffffu) | (l2 << 16), (h2 >> 16) | (l2 & 0xffff0000u), h2 & 0xffffu, h2 >> 16};
        asm volatile("" : "+v"(bx));
      }
      if constexpr (n == 58) RAWBAR();
      if constexpr (es::rf(n)) {
        constexpr int i = (n - es::RF0) / es::RFS;
        glds1(LB + (4 * i) * 1024 + w * 1024, i < 11 || w < 2 ? lane * 16 : OOB, s_ws, ring_src + (150 + 4 * i) * 1024 + w * 1024);
      }
      if constexpr (n + PD == 150) { WAITV(es::between(es::RF0 + (RFSPLIT - 1) * es::RFS, 144)); RAWBAR(); }
      if constexpr (RFSPLIT < 12 && n + PD == 150 + 4 * RFSPLIT) { WAITV(es::between(es::RFL, 144 + 4 * RFSPLIT)); RAWBAR(); }
      if constexpr (n + PD < 196) ar[(n + PD) % AR] = E_RD(n + PD);
      if constexpr (n >= 84 && n < 87) aA[n - 84] = mfma(ex[n - 84], bx, aA[n - 84]);
      if constexpr (n >= 90 && n < 102) {
        constexpr int t = (n - 90) >> 2, d = (n - 90) & 3;
        keep_acc(aA[t]);
        hv[d] = PROD(aA[t], d);
        if constexpr (d == 3) HH_ST(t, hv);
      }
      if constexpr (n == es::FA) {
        WAITV(es::between(101, es::FA));
        aflag<0>(vtag, hso, s_ws);
      }
      if constexpr (n == 140 || n == 141) aB[n & 1] = mfma(ex[3 + (n & 1)], bx, aB[n & 1]);
      if constexpr (n >= 146 && n < 154) {
        constexpr int t = (n - 146) >> 2, d = (n - 146) & 3;
        keep_acc(aB[t]);
        hv[d] = PROD(aB[t], d);
        if constexpr (d == 3) HH_ST(3 + t, hv);
      }
      static_assert(RFSPLIT < 12 && 150 + 4 * RFSPLIT - PD >= 150 && 150 + 4 * RFSPLIT - PD < es::HA0, "");
      if constexpr (n == 156) asm volatile("" : "+v"(ex[5]), "+v"(ex[6]));
      if constexpr (n == es::FH) aload32(fh, hfo, s_ws);
      if constexpr (n == es::FB) {
        await1<es::between(es::FH, es::FB)>(fh);
        aflag<64>(vtag, hso, s_ws);
        if (!__all(fh == tag)) poll_flags(ws, s_ws, hfo, tag);
      }
      if constexpr (n >= es::HA0 && n < es::HA0 + 16 && (n - es::HA0) % 2 == 0) H_LD((n - es::HA0) / 2);
      static_assert(150 + 4 * RFSPLIT == 174 && es::HA0 + 14 < 193, "");
      if constexpr (n == 184) { aC[0] = mfma(ex[5], bx, aC[0]); keep_acc(aC[0]); }
      if constexpr (n >= 187 && n < 191) {
        keep_acc(aC[0]);
        hv[n - 187] = PROD(aC[0], n - 187);
        asm volatile("" : "+v"(hv[n - 187]));
      }
      if constexpr (n == 193) HH_ST(5, hv);
      GAP();
    });
    STAMP(19); STAMPC(43);
    aC[1] = mfma(ex[6], bx, aC[1]);
    keep_acc(aC[1]);
    sfor<4>(LAM(i) { constexpr int i = i_.value; H_LD(8 + i); });
    GAP();
    sfor<4>(LAM(i) { constexpr int i = i_.value; hv[i] = PROD(aC[1], i); });
    HH_ST(6, hv);
    GAP();
    sfor<4>(LAM(i) { constexpr int i = i_.value; H_LD(12 + i); });
  }
  STAMP(20);

  if (dwg) {
    RELANE();
    u32 hb, hc;
    aload32(hb, hfo + 64, s_ws);
    f32x16 af[2];
    constexpr int PD = 6, AR = 8;
    u32x4 br[AR];
    u32 lb[2][2];
#pragma unroll
    for (int t = 0; t < 2; t++) { lb[t][0] = lane * 16 + kh * 16384 + (t ^ kh) * 8192; lb[t][1] = lb[t][0] + 65536; }
    asm volatile("" : "+v"(lb[0][0]), "+v"(lb[0][1]), "+v"(lb[1][0]), "+v"(lb[1][1]));
#define F_RD(n) LR128(AT(lb[(n) & 1], H_SLOT((n) >> 4) + (((n) >> 1) & 7) * 1024))
    sfor<8>(LAM(q) { constexpr int q = q_.value; H_LD(16 + q); });
    WAITV(8 + 2 + 1 + 8);
    RAWBAR();
    STAMP(21); STAMPC(44);
    sfor<PD>(LAM(n) { constexpr int n = n_.value; br[n] = F_RD(n); });
    GAP();
    static_assert(WD_F == 0, "");
    sfor<112>(LAM(n) {
      constexpr int n = n_.value;
      af[n & 1] = mfma(wdf[n >> 1], br[n % AR], n < 2 ? f32x16{} : af[n & 1]);
      if constexpr (n + PD < 96) asm volatile("" : "+a"(af[n & 1]));
      if constexpr (n + PD == 16) { WAITV(1 + 8); aflag<128>(vtag, hso, s_ws); RAWBAR(); }
      if constexpr (n == 16) {
        await1<9>(hb);
        if (!__all(hb == tag)) poll_flags(ws, s_ws, hfo + 64, tag);
      }
      if constexpr (n >= 16 && n < 24) H_LD(24 + n - 16);
      if constexpr (n + PD == 32) { WAITV(1 + 8); RAWBAR(); }
      if constexpr (n >= 27 && n < 35) H_LD(32 + n - 27);
      if constexpr (n + PD == 48) { WAITV(8); RAWBAR(); aload32(hc, hfo + 128, s_ws); }
      if constexpr (n + PD == 64) {
        await1<0>(hc);
        if (!__all(hc == tag)) poll_flags(ws, s_ws, hfo + 128, tag);
        RAWBAR();
      }
      if constexpr (n >= 58 && n < 74) H_LD(40 + n - 58);
      if constexpr (n + PD == 80) { WAITV(8); RAWBAR(); }
      if constexpr (n + PD == 96) { WAITV(0); RAWBAR(); }
      if constexpr (n + PD < 112) br[(n + PD) % AR] = F_RD(n + PD);
      GAP();
    });
    STAMP(22); STAMPC(45);
    {
      const f32x16& snd = af[1];
      const u32 ad = L_FEX + (w ^ 2) * 4096 + lane * 16;
#pragma unroll
      for (int g = 0; g < 4; g++) *(AS3 f32x4*)(L + ad + g * 1024) = f32x4{snd[4 * g], snd[4 * g + 1], snd[4 * g + 2], snd[4 * g + 3]};
    }
    const float rr2 = *(AS3 float*)(L + L_R2 + (tph * 64 + th * 32 + l31) * 4);
    f32x4 gb4[4];
#pragma unroll
    for (int g = 0; g < 4; g++) gb4[g] = *(AS3 f32x4*)(L + L_GB + (nh * 32 + g * 8 + 4 * hf) * 4);
    u32 oo = (mt * 128 + tph * 64 + th * 32 + l31) * 896 + (ng * 64 + nh * 32) * 2 + hf * 16;
    asm volatile("" : "+v"(oo));
    BAR();
    u32 po[8];
#pragma unroll
    for (int g = 0; g < 4; g++) {
      const f32x4 ot = *(AS3 f32x4*)(L + L_FEX + w * 4096 + lane * 16 + g * 1024);
      const f32x4 gb = gb4[g];
      float v[4];
#pragma unroll
      for (int e = 0; e < 4; e++) v[e] = (af[0][4 * g + e] + ot[e]) * rr2 * gb[e];
      const u32 p0 = x1p[g >> 1][(g & 1) * 2], p1 = x1p[g >> 1][(g & 1) * 2 + 1];
      po[2 * g] = cvtpk(blo(p0) + v[0], bhi(p0) + v[1]);
      po[2 * g + 1] = cvtpk(blo(p1) + v[2], bhi(p1) + v[3]);
    }
    const auto r_o = RSRC(out, 2048 * 896);
#pragma unroll
    for (int g2 = 0; g2 < 2; g2++) {
      auto s0 = __builtin_amdgcn_permlane32_swap(po[4 * g2], po[4 * g2 + 2], false, false);
      auto s1 = __builtin_amdgcn_permlane32_swap(po[4 * g2 + 1], po[4 * g2 + 3], false, false);
      __builtin_amdgcn_raw_buffer_store_b128(u32x4{s0[0], s1[0], s0[1], s1[1]}, r_o, oo + g2 * 32, 0, 0);
    }
  }
  else {
    WAITV(0);
    if (lane == 0) set_flag(ws, WS_HF + (mt * 4 + w) * 256 + 128 + j * 4, tag);
  }
  STAMP(23);
  STAMP_FLUSH();
}

extern "C" void kernel_launch(void* const* d_in, const int* in_sizes, int n_in, void* d_out, int out_size, void* d_ws, size_t ws_size, hipStream_t stream) {
  (void)in_sizes; (void)n_in; (void)out_size; (void)ws_size;
  (void)hipFuncSetAttribute((const void*)fused_block, hipFuncAttributeMaxDynamicSharedMemorySize, LDS_BYTES);
  fused_block<<<256, 256, LDS_BYTES, stream>>>((const u16*)d_in[0], (const u16*)d_in[1], (const u16*)d_in[2], (const u16*)d_in[3], (const u16*)d_in[5], (const u16*)d_in[6],
                                               (const u16*)d_in[7], (const u16*)d_in[8], (const u16*)d_in[9], (u16*)d_out, (char*)d_ws);
}
```
